```python
import torch

def _softmax(x, axis=-1):
    return torch.softmax(x, dim=axis)

def _gelu(x):
    return torch.nn.functional.gelu(x, approximate='tanh')

def _silu(x):
    return torch.nn.functional.silu(x)

def _sigmoid(x):
    return torch.sigmoid(x)

def _layernorm(x, axis=-1, eps=1e-05):
    m = x.mean(dim=axis, keepdim=True)
    v = x.var(dim=axis, keepdim=True, unbiased=False)
    return (x - m) / torch.sqrt(v + eps)

def _scatter_add(tgt, idx, src, axis=0):
    return tgt.clone().index_add_(axis, idx, src)

def _segment_sum(src, seg, n):
    out = torch.zeros((n,) + src.shape[1:], dtype=src.dtype)
    return out.index_add_(0, seg, src)

def _pad(x, pw):
    flat = tuple((v for pair in reversed(pw) for v in pair))
    return torch.nn.functional.pad(x, flat)
kernel_name = 'sequential_preLN__memory_aug_recurrent_mem_tr__glu_gated_geglu__prompt'

def setup_inputs():

    _w = lambda a: (lambda t: t.to(torch.bfloat16) if t.is_floating_point() else t)(torch.as_tensor(a))

    def _A_setup():
        B, S, Dm = (1, 2048, 512)
        H, Dh = (32, 128)
        M, seg = (8, 32)
        seg = min(seg, S)
        S = S // seg * seg
        return {'x': _w((lambda *s: torch.randn(*s, dtype=torch.bfloat16) * 0.05)(B, S, Dm)), 'mem0': _w((lambda *s: torch.randn(*s, dtype=torch.bfloat16) * 0.2)(B, M, Dm)), 'Wq': _w((lambda *s: torch.randn(*s, dtype=torch.bfloat16) * 1.6)(Dm, H, Dh) * Dm ** (-0.5)), 'Wk': _w((lambda *s: torch.randn(*s, dtype=torch.bfloat16) * 1.6)(Dm, H, Dh) * Dm ** (-0.5)), 'Wv': _w((lambda *s: torch.randn(*s, dtype=torch.bfloat16) * 0.2)(Dm, H, Dh) * Dm ** (-0.5)), 'Wo': _w((lambda *s: torch.randn(*s, dtype=torch.bfloat16) * 0.2)(H, Dh, Dm) * (H * Dh) ** (-0.5)), 'seg_len': seg}
    _A_in = _A_setup()

    def _B_setup():
        B, S, Dm, Df = (1, 2048, 512, 1376)
        return {'x': _w((lambda *s: torch.randn(*s, dtype=torch.bfloat16) * 0.05)(B, S, Dm)), 'w_gate': _w((lambda *s: torch.randn(*s, dtype=torch.bfloat16) * 0.4)(Dm, Df) * Dm ** (-0.5)), 'w_up': _w((lambda *s: torch.randn(*s, dtype=torch.bfloat16) * 0.4)(Dm, Df) * Dm ** (-0.5)), 'w_down': _w((lambda *s: torch.randn(*s, dtype=torch.bfloat16) * 0.4)(Df, Dm) * Df ** (-0.5))}
    _B_in = _B_setup()
    return {'A_x': _A_in['x'], 'A_mem0': _A_in['mem0'], 'A_Wq': _A_in['Wq'], 'A_Wk': _A_in['Wk'], 'A_Wv': _A_in['Wv'], 'A_Wo': _A_in['Wo'], 'A_seg_len': _A_in['seg_len'], 'B_x': _B_in['x'], 'B_w_gate': _B_in['w_gate'], 'B_w_up': _B_in['w_up'], 'B_w_down': _B_in['w_down']}

def reference(A_x, A_mem0, A_Wq, A_Wk, A_Wv, A_Wo, A_seg_len, B_x, B_w_gate, B_w_up, B_w_down):

    def _A_ref(x, mem0, Wq, Wk, Wv, Wo, seg_len):
        B, S, Dm = x.shape
        H, Dh = (Wq.shape[1], Wq.shape[2])
        M = mem0.shape[1]
        scale = Dh ** (-0.5)
        n_seg = S // seg_len
        L = M + seg_len
        idx = torch.arange(L)
        mask = (idx[:, None] < M) | (idx[:, None] >= idx[None, :])
        mem = mem0
        outs = []
        for s in range(n_seg):
            xs = x[:, s * seg_len:(s + 1) * seg_len, :].clone()
            h = torch.concatenate([mem, xs], axis=1)
            q = torch.einsum('btd,dhe->bhte', h, Wq)
            k = torch.einsum('btd,dhe->bhte', h, Wk)
            v = torch.einsum('btd,dhe->bhte', h, Wv)
            scores = torch.einsum('bhqe,bhke->bhqk', q, k) * scale
            scores = torch.where(mask[None, None], scores, -1000000000.0)
            p = _softmax(scores, axis=-1)
            o = torch.einsum('bhqk,bhke->bhqe', p, v)
            o = torch.einsum('bhte,hed->btd', o, Wo)
            mem = o[:, :M, :].clone()
            outs.append(o[:, M:, :])
        return torch.concatenate(outs, axis=1)

    def _B_ref(x, w_gate, w_up, w_down):
        g = torch.einsum('bsd,df->bsf', x, w_gate)
        u = torch.einsum('bsd,df->bsf', x, w_up)
        h = _gelu(g) * u
        return torch.einsum('bsf,fd->bsd', h, w_down)
    x = A_x
    h = x + _A_ref(_layernorm(x, axis=-1), A_mem0, A_Wq, A_Wk, A_Wv, A_Wo, A_seg_len)
    h2 = h + _B_ref(_layernorm(h, axis=-1), B_w_gate, B_w_up, B_w_down)
    return h2
```

```cpp
#include <hip/hip_runtime.h>
#include <hip/hip_bf16.h>
#include <stdint.h>

typedef __bf16 bf16;
typedef __bf16 bf16x2 __attribute__((ext_vector_type(2)));
typedef __bf16 bf16x4 __attribute__((ext_vector_type(4)));
typedef __bf16 bf16x8 __attribute__((ext_vector_type(8)));
typedef __bf16 bf16x16 __attribute__((ext_vector_type(16)));
typedef short s16x4 __attribute__((ext_vector_type(4)));
typedef float f32x2 __attribute__((ext_vector_type(2)));
typedef float f32x4 __attribute__((ext_vector_type(4)));
typedef unsigned u32x2 __attribute__((ext_vector_type(2)));
typedef unsigned u32x4 __attribute__((ext_vector_type(4)));
typedef unsigned u32x8 __attribute__((ext_vector_type(8)));
#define LAS __attribute__((address_space(3)))
#define DI __device__ __forceinline__

constexpr int S_ = 2048, DM = 512, NH = 32, DH = 128, HD = NH * DH, MM = 8, SEG = 32, NSEG = S_ / SEG, DF = 1376, DFP = 1408;

constexpr size_t WS_PART = 0;
constexpr size_t WS_MBUF = WS_PART + 2u * 524288u;
constexpr size_t WS_XN   = WS_MBUF + 64u * 8192u;
constexpr size_t WS_Q    = WS_XN + (size_t)S_ * DM * 2;
constexpr size_t WS_K    = WS_Q + (size_t)S_ * HD * 2;
constexpr size_t WS_VT   = WS_K + (size_t)S_ * HD * 2;
constexpr size_t WS_KM   = WS_VT + (size_t)S_ * HD * 2;
constexpr size_t WS_VMT  = WS_KM + (size_t)NSEG * MM * HD * 2;
constexpr size_t WS_O    = WS_VMT + (size_t)NSEG * MM * HD * 2;
constexpr size_t WS_P    = WS_O + (size_t)S_ * HD * 2;
constexpr size_t WS_H    = WS_P + (size_t)4 * S_ * DM * 4;
constexpr size_t WS_HN   = WS_H + (size_t)S_ * DM * 2;
constexpr size_t WS_ACT  = WS_HN + (size_t)S_ * DM * 2;
constexpr size_t WS_END  = WS_ACT + (size_t)S_ * DFP * 2;

DI unsigned pk2(float a, float b) { f32x2 f = {a, b}; return __builtin_bit_cast(unsigned, __builtin_convertvector(f, bf16x2)); }
DI float bflo(unsigned u) { return __uint_as_float(u << 16); }
DI float bfhi(unsigned u) { return __uint_as_float(u & 0xffff0000u); }
DI f32x4 mfma16(bf16x8 a, bf16x8 b, f32x4 c) { return __builtin_amdgcn_mfma_f32_16x16x32_bf16(a, b, c, 0, 0, 0); }
DI f32x4 smf(bf16x8 a, bf16x16 b, f32x4 c, int idx) { return __builtin_amdgcn_smfmac_f32_16x16x64_bf16(a, b, c, idx, 0, 0); }
DI bf16x8 mk8(unsigned a, unsigned b, unsigned c, unsigned d) { u32x4 u = {a, b, c, d}; return __builtin_bit_cast(bf16x8, u); }
DI u32x2 trread(const char* lds_ptr) {
  s16x4 r = __builtin_amdgcn_ds_read_tr16_b64_v4i16((LAS s16x4*)lds_ptr);
  return __builtin_bit_cast(u32x2, r);
}
DI float xmax32(float x) { auto r = __builtin_amdgcn_permlane32_swap(__float_as_uint(x), __float_as_uint(x), false, false); return fmaxf(__uint_as_float(r[0]), __uint_as_float(r[1])); }
DI float xmax16(float x) { auto r = __builtin_amdgcn_permlane16_swap(__float_as_uint(x), __float_as_uint(x), false, false); return fmaxf(__uint_as_float(r[0]), __uint_as_float(r[1])); }
DI float xsum32(float x) { auto r = __builtin_amdgcn_permlane32_swap(__float_as_uint(x), __float_as_uint(x), false, false); return __uint_as_float(r[0]) + __uint_as_float(r[1]); }
DI float xsum16(float x) { auto r = __builtin_amdgcn_permlane16_swap(__float_as_uint(x), __float_as_uint(x), false, false); return __uint_as_float(r[0]) + __uint_as_float(r[1]); }
DI float comb(float a, float b) { auto r = __builtin_amdgcn_permlane32_swap(__float_as_uint(a), __float_as_uint(b), false, false); return __uint_as_float(r[0]) + __uint_as_float(r[1]); }

__global__ void __launch_bounds__(256) prep_kernel(const bf16* __restrict__ x, const bf16* __restrict__ mem0, char* ws) {
  const int t = threadIdx.x, lane = t & 63, w = t >> 6;
  const int row = blockIdx.x * 4 + w;
  {
    u32x4 v = *(const u32x4*)(x + (size_t)row * DM + lane * 8);
    float f[8] = {bflo(v[0]), bfhi(v[0]), bflo(v[1]), bfhi(v[1]), bflo(v[2]), bfhi(v[2]), bflo(v[3]), bfhi(v[3])};
    float s = 0;
#pragma unroll
    for (int i = 0; i < 8; ++i) s += f[i];
#pragma unroll
    for (int m = 32; m >= 1; m >>= 1) s += __shfl_xor(s, m);
    const float mean = s * (1.f / DM);
    float q = 0;
#pragma unroll
    for (int i = 0; i < 8; ++i) { f[i] -= mean; q += f[i] * f[i]; }
#pragma unroll
    for (int m = 32; m >= 1; m >>= 1) q += __shfl_xor(q, m);
    const float rs = 1.f / sqrtf(q * (1.f / DM) + 1e-5f);
    u32x4 o = {pk2(f[0] * rs, f[1] * rs), pk2(f[2] * rs, f[3] * rs), pk2(f[4] * rs, f[5] * rs), pk2(f[6] * rs, f[7] * rs)};
    *(u32x4*)(ws + WS_XN + ((size_t)row * DM + lane * 8) * 2) = o;
  }
  const int gt = blockIdx.x * 256 + t;
  if (gt < 65536) { u32x4 z = {0, 0, 0, 0}; *(u32x4*)(ws + WS_PART + (size_t)gt * 16) = z; }
  else if (gt < 65536 + 32768) {
    const int u = gt - 65536;
    u32x4 z = {~0u, ~0u, ~0u, ~0u};
    if (u < 512) {
      const int ks = u >> 6, ga = (u >> 4) & 3, hb = (u >> 3) & 1, i = u & 7;
      z = *(const u32x4*)(mem0 + i * DM + ks * 64 + ga * 16 + hb * 8);
#pragma unroll
      for (int j = 0; j < 4; ++j) if (z[j] == ~0u) z[j] = 0x7fff7fffu;
    }
    *(u32x4*)(ws + WS_MBUF + (size_t)u * 16) = z;
  }
  if (gt < S_ * 4) { const int r = gt >> 2, c = gt & 3; u32x4 z = {0, 0, 0, 0}; *(u32x4*)(ws + WS_ACT + ((size_t)r * DFP + DF + c * 8) * 2) = z; }
}

DI unsigned off_b(unsigned row, unsigned ch) { return 256u * row + 16u * (ch ^ (((row & 3) << 2) | ((row >> 2) & 3))); }

template <bool WA, class BCol>
DI void gemm_tile(const bf16* __restrict__ A, int lda, const bf16* __restrict__ B, int ldb, BCol bcol, int kbeg, int nkt, int kmaxB,
                  char* lds, f32x4 (&acc)[4][4]) {
  const int t = threadIdx.x, lane = t & 63, w = t >> 6, wr = w >> 1, wc = w & 1, c = lane & 15, g = lane >> 4;
  const bf16* asrc[4]; const bf16* bsrc[4]; int brow[4];
#pragma unroll
  for (int i = 0; i < 4; ++i) {
    const int u = i * 256 + t;
    const int r = u >> 3, sl = u & 7;
    asrc[i] = A + (size_t)r * lda + kbeg + ((sl ^ (r & 7)) << 3);
    const int kr = u >> 4, sb = u & 15;
    const int ch = sb ^ (((kr & 3) << 2) | ((kr >> 2) & 3));
    brow[i] = kbeg + kr;
    bsrc[i] = B + bcol(ch);
  }
  auto stage = [&](int kt, int buf) {
    char* la = lds + buf * 32768; char* lb = la + 16384;
#pragma unroll
    for (int i = 0; i < 4; ++i) {
      __builtin_amdgcn_global_load_lds((const unsigned*)(asrc[i] + kt * 64), (LAS unsigned*)(la + (i * 256 + w * 64) * 16), 16, 0, 0);
      int kr = brow[i] + kt * 64; kr = kr < kmaxB ? kr : kmaxB;
      __builtin_amdgcn_global_load_lds((const unsigned*)(bsrc[i] + (size_t)kr * ldb), (LAS unsigned*)(lb + (i * 256 + w * 64) * 16), 16, 0, 0);
    }
  };
#pragma unroll
  for (int i = 0; i < 4; ++i)
#pragma unroll
    for (int j = 0; j < 4; ++j) acc[i][j] = (f32x4){0, 0, 0, 0};
  stage(0, 0);
  __syncthreads();
  const int q = c >> 2, p = c & 3;
  for (int kt = 0; kt < nkt; ++kt) {
    const int buf = kt & 1;
    if (kt + 1 < nkt) stage(kt + 1, buf ^ 1);
    const char* la = lds + buf * 32768; const char* lb = la + 16384;
#pragma unroll
    for (int ks = 0; ks < 2; ++ks) {
      bf16x8 af[4], bfr[4];
#pragma unroll
      for (int mi = 0; mi < 4; ++mi) {
        const int r = wr * 64 + mi * 16 + c;
        af[mi] = *(const bf16x8*)(la + r * 128 + (((ks * 4 + g) ^ (r & 7)) << 4));
      }
#pragma unroll
      for (int ni = 0; ni < 4; ++ni) {
        const int cc = wc * 4 + ni;
        u32x2 lo = trread(lb + off_b(32 * ks + 8 * g + q, 2 * cc + (p >> 1)) + 8 * (p & 1));
        u32x2 hi = trread(lb + off_b(32 * ks + 8 * g + 4 + q, 2 * cc + (p >> 1)) + 8 * (p & 1));
        bfr[ni] = mk8(lo[0], lo[1], hi[0], hi[1]);
      }
#pragma unroll
      for (int mi = 0; mi < 4; ++mi)
#pragma unroll
        for (int ni = 0; ni < 4; ++ni)
          acc[mi][ni] = WA ? mfma16(bfr[ni], af[mi], acc[mi][ni]) : mfma16(af[mi], bfr[ni], acc[mi][ni]);
    }
    __syncthreads();
  }
}

__global__ void __launch_bounds__(256) qkv_kernel(const bf16* __restrict__ Wq, const bf16* __restrict__ Wk, const bf16* __restrict__ Wv, char* ws) {
  extern __shared__ __attribute__((aligned(16))) char lds[];
  const int bid = blockIdx.x;
  const int mt = bid & 15, nt = bid >> 4;
  const int mat = nt >> 5, h = nt & 31;
  const bf16* W = mat == 0 ? Wq : (mat == 1 ? Wk : Wv);
  const bf16* A = (const bf16*)(ws + WS_XN) + (size_t)mt * 128 * DM;
  const int t = threadIdx.x, lane = t & 63, w = t >> 6, wr = w >> 1, wc = w & 1, c = lane & 15, g = lane >> 4;
  f32x4 acc[4][4];
  auto bcol = [&](int ch) { return h * 128 + ch * 8; };
  if (mat < 2) {
    gemm_tile<true>(A, DM, W, HD, bcol, 0, DM / 64, DM - 1, lds, acc);
    bf16* O = (bf16*)(ws + (mat == 0 ? WS_Q : WS_K));
#pragma unroll
    for (int mi = 0; mi < 4; ++mi)
#pragma unroll
      for (int ni = 0; ni < 4; ++ni) {
        const int tok = mt * 128 + wr * 64 + mi * 16 + c, d = wc * 64 + ni * 16 + 4 * g;
        u32x2 o = {pk2(acc[mi][ni][0], acc[mi][ni][1]), pk2(acc[mi][ni][2], acc[mi][ni][3])};
        *(u32x2*)(O + (size_t)tok * HD + h * 128 + d) = o;
      }
  } else {
    gemm_tile<false>(A, DM, W, HD, bcol, 0, DM / 64, DM - 1, lds, acc);
    bf16* O = (bf16*)(ws + WS_VT);
#pragma unroll
    for (int mi = 0; mi < 4; ++mi)
#pragma unroll
      for (int ni = 0; ni < 4; ++ni) {
        const int tok0 = mt * 128 + wr * 64 + mi * 16;
        const int seg = tok0 >> 5, xt = (tok0 >> 4) & 1, d = wc * 64 + ni * 16 + c;
        u32x2 o = {pk2(acc[mi][ni][0], acc[mi][ni][1]), pk2(acc[mi][ni][2], acc[mi][ni][3])};
        *(u32x2*)(O + (((size_t)seg * NH + h) * DH + d) * 32 + g * 8 + xt * 4) = o;
      }
  }
}

constexpr int CL_WV = 0, CL_M = 131072, CL_QK = CL_M + 8192, CL_SP = CL_QK + 4096, CL_O = CL_SP + 12288, CL_R = CL_O + 2048, CL_END = CL_R + 2048;
#define DLY1 8
#define DLY2 10
constexpr unsigned SPIN_CAP = 1u << 18;

DI void st16_sc1(void* p, u32x4 v) { asm volatile("global_store_dwordx4 %0, %1, off sc1\n\ts_nop 1" ::"v"(p), "v"(v) : "memory"); }
DI void st4_sc1(void* p, unsigned v) { asm volatile("global_store_dword %0, %1, off sc1" ::"v"(p), "v"(v) : "memory"); }

DI int wrow(int ks, int d, int gb) { return 64 * ks + 16 * (2 * (d >> 2) + (gb >> 1)) + 8 * (d & 1) + 2 * (2 * (gb & 1) + ((d >> 1) & 1)); }

__global__ void __launch_bounds__(256, 1) chain_kernel(const bf16* __restrict__ Wq, const bf16* __restrict__ Wk, const bf16* __restrict__ Wv,
                                                       const bf16* __restrict__ Wo, char* ws) {
  extern __shared__ __attribute__((aligned(16))) char lds[];
  const int h = blockIdx.x;
  const int t = threadIdx.x, lane = t & 63, w = __builtin_amdgcn_readfirstlane(t >> 6), c = lane & 15, g = lane >> 4;
  u32x8 wq[2][8], wk[2][8], wo[8][2];
  {
    const int col = h * 128 + w * 32 + 2 * c;
#pragma unroll
    for (int ks = 0; ks < 8; ++ks) {
#pragma unroll
      for (int d = 0; d < 8; ++d) {
        const int r = wrow(ks, d, g);
        unsigned a = *(const unsigned*)(Wq + (size_t)r * HD + col), b = *(const unsigned*)(Wq + (size_t)(r + 1) * HD + col);
        wq[0][ks][d] = __builtin_amdgcn_perm(b, a, 0x05040100u); wq[1][ks][d] = __builtin_amdgcn_perm(b, a, 0x07060302u);
        a = *(const unsigned*)(Wk + (size_t)r * HD + col); b = *(const unsigned*)(Wk + (size_t)(r + 1) * HD + col);
        wk[0][ks][d] = __builtin_amdgcn_perm(b, a, 0x05040100u); wk[1][ks][d] = __builtin_amdgcn_perm(b, a, 0x07060302u);
      }
      u32x8 v0, v1;
#pragma unroll
      for (int d = 0; d < 8; ++d) {
        const int r = wrow(ks, d, g);
        unsigned a = *(const unsigned*)(Wv + (size_t)r * HD + col), b = *(const unsigned*)(Wv + (size_t)(r + 1) * HD + col);
        v0[d] = __builtin_amdgcn_perm(b, a, 0x05040100u); v1[d] = __builtin_amdgcn_perm(b, a, 0x07060302u);
      }
      char* p0 = lds + CL_WV + (((w * 2 + 0) * 8 + ks) * 2) * 1024 + lane * 16;
      char* p1 = lds + CL_WV + (((w * 2 + 1) * 8 + ks) * 2) * 1024 + lane * 16;
      *(u32x4*)p0 = (u32x4){v0[0], v0[1], v0[2], v0[3]}; *(u32x4*)(p0 + 1024) = (u32x4){v0[4], v0[5], v0[6], v0[7]};
      *(u32x4*)p1 = (u32x4){v1[0], v1[1], v1[2], v1[3]}; *(u32x4*)(p1 + 1024) = (u32x4){v1[4], v1[5], v1[6], v1[7]};
    }
#pragma unroll
    for (int ks = 0; ks < 2; ++ks)
#pragma unroll
      for (int d = 0; d < 8; ++d) {
        const int r = h * 128 + wrow(ks, d, g);
        u32x4 a = *(const u32x4*)(Wo + (size_t)r * DM + w * 128 + c * 8), b = *(const u32x4*)(Wo + (size_t)(r + 1) * DM + w * 128 + c * 8);
#pragma unroll
        for (int j = 0; j < 4; ++j) { wo[2 * j][ks][d] = __builtin_amdgcn_perm(b[j], a[j], 0x05040100u); wo[2 * j + 1][ks][d] = __builtin_amdgcn_perm(b[j], a[j], 0x07060302u); }
      }
  }
  const int sidx = (c & 8) ? 0xEEEE : 0x4444;
  const bf16* Kx = (const bf16*)(ws + WS_K);
  const bf16* Vt = (const bf16*)(ws + WS_VT);
  bf16* km = (bf16*)(ws + WS_KM);
  bf16* vmT = (bf16*)(ws + WS_VMT);
  const float sc = 0.08838834764831845f * 1.4426950408889634f;
  bool dead = false;

  for (int s = 0; s < NSEG; ++s) {
    {
      const char* src = ws + WS_MBUF + (size_t)s * 8192 + t * 16;
      u32x4 a, b;
      for (unsigned spins = 0;; ++spins) {
        asm volatile("global_load_dwordx4 %0, %2, off sc1\n\tglobal_load_dwordx4 %1, %3, off sc1\n\ts_waitcnt vmcnt(0)"
                     : "=&v"(a), "=&v"(b) : "v"(src), "v"(src + 4096) : "memory");
        bool ok = (a[0] != ~0u) & (a[1] != ~0u) & (a[2] != ~0u) & (a[3] != ~0u) & (b[0] != ~0u) & (b[1] != ~0u) & (b[2] != ~0u) & (b[3] != ~0u);
        if (__all(ok) || dead) break;
        if (spins > SPIN_CAP) { dead = true; break; }
      }
      *(u32x4*)(lds + CL_M + t * 16) = a;
      *(u32x4*)(lds + CL_M + 4096 + t * 16) = b;
    }
    bf16x8 kx[2], vx[2];
#pragma unroll
    for (int i = 0; i < 2; ++i) {
      kx[i] = *(const bf16x8*)(Kx + (size_t)(s * 32 + 16 * i + c) * HD + h * 128 + w * 32 + 8 * g);
      vx[i] = *(const bf16x8*)(Vt + (((size_t)s * NH + h) * DH + w * 32 + 2 * c + i) * 32 + 8 * g);
    }
    __syncthreads();
    f32x4 aq[2], ak[2], av[2];
#pragma unroll
    for (int i = 0; i < 2; ++i) { aq[i] = (f32x4){0, 0, 0, 0}; ak[i] = aq[i]; av[i] = aq[i]; }
    bf16x8 mf[8];
#pragma unroll
    for (int ks = 0; ks < 8; ++ks) mf[ks] = *(const bf16x8*)(lds + CL_M + ks * 1024 + lane * 16);
#pragma unroll
    for (int ks = 0; ks < 8; ++ks) {
#pragma unroll
      for (int i = 0; i < 2; ++i) {
        aq[i] = smf(mf[ks], __builtin_bit_cast(bf16x16, wq[i][ks]), aq[i], sidx);
        ak[i] = smf(mf[ks], __builtin_bit_cast(bf16x16, wk[i][ks]), ak[i], sidx);
      }
    }
    char* qt = lds + CL_QK + w * 1024; char* kt = qt + 512;
    {
      const int off = (2 * c + (lane >> 5)) * 16 + (g & 1) * 8;
      u32x2 qq = {pk2(comb(aq[0][0], aq[1][0]), comb(aq[0][1], aq[1][1])), pk2(comb(aq[0][2], aq[1][2]), comb(aq[0][3], aq[1][3]))};
      u32x2 kk = {pk2(comb(ak[0][0], ak[1][0]), comb(ak[0][1], ak[1][1])), pk2(comb(ak[0][2], ak[1][2]), comb(ak[0][3], ak[1][3]))};
      *(u32x2*)(qt + off) = qq; *(u32x2*)(kt + off) = kk;
    }
#pragma unroll
    for (int ks = 0; ks < 8; ++ks) {
#pragma unroll
      for (int i = 0; i < 2; ++i) {
        const char* p = lds + CL_WV + (((w * 2 + i) * 8 + ks) * 2) * 1024 + lane * 16;
        u32x4 lo = *(const u32x4*)p, hi = *(const u32x4*)(p + 1024);
        u32x8 wv = {lo[0], lo[1], lo[2], lo[3], hi[0], hi[1], hi[2], hi[3]};
        av[i] = smf(mf[ks], __builtin_bit_cast(bf16x16, wv), av[i], sidx);
      }
    }
    bf16x8 qf, kf;
    {
      const int q4 = c >> 2, p = c & 3;
      const int off = (8 * g + q4) * 16 + (p & 1) * 8;
      u32x2 a = trread(qt + off), b = trread(qt + off + 64);
      qf = mk8(a[0], a[1], b[0], b[1]);
      a = trread(kt + off); b = trread(kt + off + 64);
      kf = mk8(a[0], a[1], b[0], b[1]);
    }
    if (c < 8) *(bf16x8*)(km + (size_t)(s * 8 + c) * HD + h * 128 + w * 32 + 8 * g) = kf;
    {
      u32x2 vv = {pk2(comb(av[0][0], av[1][0]), comb(av[0][1], av[1][1])), pk2(comb(av[0][2], av[1][2]), comb(av[0][3], av[1][3]))};
      *(u32x2*)(vmT + (((size_t)s * NH + h) * DH + w * 32 + 2 * c + (lane >> 5)) * 8 + (g & 1) * 4) = vv;
    }
    if (s == NSEG - 1) break;
    {
      const f32x4 z = {0, 0, 0, 0};
      f32x4 s0 = mfma16(kf, qf, z), s1 = mfma16(kx[0], qf, z), s2 = mfma16(kx[1], qf, z);
      char* sp = lds + CL_SP + w * 3072 + lane * 16;
      *(f32x4*)sp = s0; *(f32x4*)(sp + 1024) = s1; *(f32x4*)(sp + 2048) = s2;
    }
    __syncthreads();
    f32x4 sm[3];
#pragma unroll
    for (int j = 0; j < 3; ++j) {
      const char* sp = lds + CL_SP + j * 1024 + lane * 16;
      sm[j] = *(const f32x4*)sp + *(const f32x4*)(sp + 3072) + (*(const f32x4*)(sp + 6144) + *(const f32x4*)(sp + 9216));
    }
    float mx = fmaxf(fmaxf(fmaxf(sm[0][0], sm[0][1]), fmaxf(sm[0][2], sm[0][3])), fmaxf(fmaxf(fmaxf(sm[1][0], sm[1][1]), fmaxf(sm[1][2], sm[1][3])), fmaxf(fmaxf(sm[2][0], sm[2][1]), fmaxf(sm[2][2], sm[2][3]))));
    mx = xmax16(mx); mx = xmax32(mx);
#pragma unroll
    for (int j = 0; j < 3; ++j)
#pragma unroll
      for (int r = 0; r < 4; ++r) sm[j][r] = __builtin_amdgcn_exp2f((sm[j][r] - mx) * sc);
    float sum = (sm[1][0] + sm[1][1]) + (sm[1][2] + sm[1][3]) + (sm[2][0] + sm[2][1]) + (sm[2][2] + sm[2][3]);
    if (g < 2) sum += (sm[0][0] + sm[0][1]) + (sm[0][2] + sm[0][3]);
    sum = xsum16(sum); sum = xsum32(sum);
    const float inv = __builtin_amdgcn_rcpf(sum);
    const bf16x8 px = mk8(pk2(sm[1][0] * inv, sm[1][1] * inv), pk2(sm[1][2] * inv, sm[1][3] * inv), pk2(sm[2][0] * inv, sm[2][1] * inv), pk2(sm[2][2] * inv, sm[2][3] * inv));
    const bf16x8 pm = mk8(pk2(sm[0][0] * inv, sm[0][1] * inv), pk2(sm[0][2] * inv, sm[0][3] * inv), 0u, 0u);
    f32x4 ot[2];
#pragma unroll
    for (int i = 0; i < 2; ++i) {
      const bf16x8 vm = mk8(pk2(av[i][0], av[i][1]), pk2(av[i][2], av[i][3]), 0u, 0u);
      ot[i] = mfma16(vx[i], px, (f32x4){0, 0, 0, 0});
      ot[i] = mfma16(vm, pm, ot[i]);
    }
    if (c < 8) {
      u32x4 o = {pk2(ot[0][0], ot[1][0]), pk2(ot[0][1], ot[1][1]), pk2(ot[0][2], ot[1][2]), pk2(ot[0][3], ot[1][3])};
      *(u32x4*)(lds + CL_O + (w >> 1) * 1024 + (16 * (2 * (w & 1) + (g >> 1)) + 8 * (g & 1) + c) * 16) = o;
    }
    __syncthreads();
    {
      const bf16x8 of0 = *(const bf16x8*)(lds + CL_O + lane * 16), of1 = *(const bf16x8*)(lds + CL_O + 1024 + lane * 16);
      f32x4 d[8];
#pragma unroll
      for (int T = 0; T < 8; ++T) {
        d[T] = smf(of0, __builtin_bit_cast(bf16x16, wo[T][0]), (f32x4){0, 0, 0, 0}, sidx);
        d[T] = smf(of1, __builtin_bit_cast(bf16x16, wo[T][1]), d[T], sidx);
      }
      const unsigned tag = (unsigned)(s + 1);
      char* dst = ws + WS_PART + (size_t)(s & 1) * 524288 + ((size_t)(8 * w + (c >> 1)) * 32 + h) * 512 + ((c & 1) * 4 + (lane >> 5) * 2 + (g & 1)) * 16;
#pragma unroll
      for (int tp = 0; tp < 4; ++tp) {
        u32x4 v;
#pragma unroll
        for (int r = 0; r < 4; ++r) v[r] = (__float_as_uint(comb(d[2 * tp][r], d[2 * tp + 1][r])) & ~127u) | tag;
        st16_sc1(dst + tp * 128, v);
      }
    }
#pragma unroll 1
    for (int k = 0; k < DLY1; ++k) __builtin_amdgcn_s_sleep(1);
    {
      char* src = ws + WS_PART + (size_t)(s & 1) * 524288 + (size_t)h * 16384 + t * 16;
      u32x4 r0, r1, r2, r3;
      const unsigned tag = (unsigned)(s + 1);
      for (unsigned spins = 0;; ++spins) {
        asm volatile("global_load_dwordx4 %0, %4, off sc1\n\tglobal_load_dwordx4 %1, %5, off sc1\n\tglobal_load_dwordx4 %2, %6, off sc1\n\tglobal_load_dwordx4 %3, %7, off sc1\n\ts_waitcnt vmcnt(0)"
                     : "=&v"(r0), "=&v"(r1), "=&v"(r2), "=&v"(r3) : "v"(src), "v"(src + 4096), "v"(src + 8192), "v"(src + 12288) : "memory");
        unsigned bad = 0;
#pragma unroll
        for (int j = 0; j < 4; ++j) bad |= (r0[j] ^ tag) | (r1[j] ^ tag) | (r2[j] ^ tag) | (r3[j] ^ tag);
        if (__all((bad & 127u) == 0) || dead) break;
        if (spins > SPIN_CAP) { dead = true; break; }
      }
      f32x4 x;
#pragma unroll
      for (int j = 0; j < 4; ++j) {
        float v = (__uint_as_float(r0[j] & ~127u) + __uint_as_float(r1[j] & ~127u)) + (__uint_as_float(r2[j] & ~127u) + __uint_as_float(r3[j] & ~127u));
        x[j] = xsum32(v);
      }
      if (lane < 32) *(f32x4*)(lds + CL_R + w * 512 + lane * 16) = x;
      __syncthreads();
      if (w == 0) {
        const int hb = lane >> 5, i = (lane >> 2) & 7, cp = lane & 3;
        const float* R = (const float*)(lds + CL_R) + (cp * 8 + hb * 4 + (i >> 2)) * 4 + (i & 3);
        const float a = (R[0] + R[128]) + (R[256] + R[384]);
        const float b = (R[8] + R[136]) + (R[264] + R[392]);
        unsigned o = pk2(a, b);
        if (o == ~0u) o = 0x7fff7fffu;
        st4_sc1(ws + WS_MBUF + (size_t)(s + 1) * 8192 + h * 256 + lane * 4, o);
      }
      const u32x4 z = {0, 0, 0, 0};
      st16_sc1(src, z); st16_sc1(src + 4096, z); st16_sc1(src + 8192, z); st16_sc1(src + 12288, z);
    }
#pragma unroll 1
    for (int k = 0; k < DLY2; ++k) __builtin_amdgcn_s_sleep(1);
  }
}

__global__ void __launch_bounds__(256) attn_kernel(char* ws) {
  const int t = threadIdx.x, lane = t & 63, w = t >> 6, c = lane & 15, g = lane >> 4;
  const int id = blockIdx.x * 4 + w, s = id >> 5, h = id & 31;
  const bf16* Q = (const bf16*)(ws + WS_Q); const bf16* K = (const bf16*)(ws + WS_K); const bf16* Vt = (const bf16*)(ws + WS_VT);
  const bf16* km = (const bf16*)(ws + WS_KM); const bf16* vmT = (const bf16*)(ws + WS_VMT);
  bf16* O = (bf16*)(ws + WS_O);
  f32x4 st[3][2];
#pragma unroll
  for (int j = 0; j < 3; ++j) { st[j][0] = (f32x4){0, 0, 0, 0}; st[j][1] = st[j][0]; }
#pragma unroll
  for (int ks = 0; ks < 4; ++ks) {
    const int col = h * 128 + ks * 32 + 8 * g;
    bf16x8 q0 = *(const bf16x8*)(Q + (size_t)(s * 32 + c) * HD + col), q1 = *(const bf16x8*)(Q + (size_t)(s * 32 + 16 + c) * HD + col);
    bf16x8 k0 = *(const bf16x8*)(K + (size_t)(s * 32 + c) * HD + col), k1 = *(const bf16x8*)(K + (size_t)(s * 32 + 16 + c) * HD + col);
    bf16x8 kmf = *(const bf16x8*)(km + (size_t)(s * 8 + (c & 7)) * HD + col);
    st[0][0] = mfma16(kmf, q0, st[0][0]); st[0][1] = mfma16(kmf, q1, st[0][1]);
    st[1][0] = mfma16(k0, q0, st[1][0]); st[1][1] = mfma16(k0, q1, st[1][1]);
    st[2][1] = mfma16(k1, q1, st[2][1]);
  }
  const float sc = 0.08838834764831845f * 1.4426950408889634f;
  bf16x8 px[2], pm[2];
#pragma unroll
  for (int qt = 0; qt < 2; ++qt) {
    float v[12];
#pragma unroll
    for (int r = 0; r < 4; ++r) {
      v[r] = (g < 2) ? st[0][qt][r] : -INFINITY;
      const int kj = 4 * g + r;
      v[4 + r] = (qt == 1 || kj <= c) ? st[1][qt][r] : -INFINITY;
      v[8 + r] = (qt == 1 && kj <= c) ? st[2][qt][r] : -INFINITY;
    }
    float mx = v[0];
#pragma unroll
    for (int i = 1; i < 12; ++i) mx = fmaxf(mx, v[i]);
    mx = xmax16(mx); mx = xmax32(mx);
    float sum = 0;
#pragma unroll
    for (int i = 0; i < 12; ++i) { v[i] = __builtin_amdgcn_exp2f((v[i] - mx) * sc); sum += v[i]; }
    sum = xsum16(sum); sum = xsum32(sum);
    const float inv = __builtin_amdgcn_rcpf(sum);
    px[qt] = mk8(pk2(v[4] * inv, v[5] * inv), pk2(v[6] * inv, v[7] * inv), pk2(v[8] * inv, v[9] * inv), pk2(v[10] * inv, v[11] * inv));
    pm[qt] = mk8(pk2(v[0] * inv, v[1] * inv), pk2(v[2] * inv, v[3] * inv), 0u, 0u);
  }
#pragma unroll
  for (int dt = 0; dt < 8; ++dt) {
    const size_t drow = ((size_t)s * NH + h) * DH + dt * 16 + c;
    bf16x8 vx = *(const bf16x8*)(Vt + drow * 32 + 8 * g);
    u32x2 vmr = *(const u32x2*)(vmT + drow * 8 + (g & 1) * 4);
    bf16x8 vm = mk8(vmr[0], vmr[1], 0u, 0u);
#pragma unroll
    for (int qt = 0; qt < 2; ++qt) {
      f32x4 o = mfma16(vx, px[qt], (f32x4){0, 0, 0, 0});
      o = mfma16(vm, pm[qt], o);
      u32x2 ov = {pk2(o[0], o[1]), pk2(o[2], o[3])};
      *(u32x2*)(O + (size_t)(s * 32 + qt * 16 + c) * HD + h * 128 + dt * 16 + 4 * g) = ov;
    }
  }
}

__global__ void __launch_bounds__(256) wo_kernel(const bf16* __restrict__ Wo, char* ws) {
  extern __shared__ __attribute__((aligned(16))) char lds[];
  const int bid = blockIdx.x, mt = bid & 15, nt = (bid >> 4) & 3, sk = bid >> 6;
  const bf16* A = (const bf16*)(ws + WS_O) + (size_t)mt * 128 * HD;
  const int t = threadIdx.x, lane = t & 63, w = t >> 6, wr = w >> 1, wc = w & 1, c = lane & 15, g = lane >> 4;
  f32x4 acc[4][4];
  auto bcol = [&](int ch) { return nt * 128 + ch * 8; };
  gemm_tile<true>(A, HD, Wo, DM, bcol, sk * 1024, 16, HD - 1, lds, acc);
  float* P = (float*)(ws + WS_P) + (size_t)sk * S_ * DM;
#pragma unroll
  for (int mi = 0; mi < 4; ++mi)
#pragma unroll
    for (int ni = 0; ni < 4; ++ni) {
      const int tok = mt * 128 + wr * 64 + mi * 16 + c, n = nt * 128 + wc * 64 + ni * 16 + 4 * g;
      *(f32x4*)(P + (size_t)tok * DM + n) = acc[mi][ni];
    }
}

__global__ void __launch_bounds__(256) ln2_kernel(const bf16* __restrict__ x, char* ws) {
  const int t = threadIdx.x, lane = t & 63, w = t >> 6;
  const int row = blockIdx.x * 4 + w;
  const float* P = (const float*)(ws + WS_P) + (size_t)row * DM + lane * 8;
  float a[8];
#pragma unroll
  for (int i = 0; i < 8; ++i) a[i] = 0;
#pragma unroll
  for (int k = 0; k < 4; ++k) {
    f32x4 p0 = *(const f32x4*)(P + (size_t)k * S_ * DM), p1 = *(const f32x4*)(P + (size_t)k * S_ * DM + 4);
#pragma unroll
    for (int i = 0; i < 4; ++i) { a[i] += p0[i]; a[4 + i] += p1[i]; }
  }
  u32x4 v = *(const u32x4*)(x + (size_t)row * DM + lane * 8);
  u32x4 hb;
#pragma unroll
  for (int i = 0; i < 4; ++i) {
    unsigned ar = pk2(a[2 * i], a[2 * i + 1]);
    hb[i] = pk2(bflo(v[i]) + bflo(ar), bfhi(v[i]) + bfhi(ar));
  }
  *(u32x4*)(ws + WS_H + ((size_t)row * DM + lane * 8) * 2) = hb;
  float f[8] = {bflo(hb[0]), bfhi(hb[0]), bflo(hb[1]), bfhi(hb[1]), bflo(hb[2]), bfhi(hb[2]), bflo(hb[3]), bfhi(hb[3])};
  float s = 0;
#pragma unroll
  for (int i = 0; i < 8; ++i) s += f[i];
#pragma unroll
  for (int m = 32; m >= 1; m >>= 1) s += __shfl_xor(s, m);
  const float mean = s * (1.f / DM);
  float q = 0;
#pragma unroll
  for (int i = 0; i < 8; ++i) { f[i] -= mean; q += f[i] * f[i]; }
#pragma unroll
  for (int m = 32; m >= 1; m >>= 1) q += __shfl_xor(q, m);
  const float rs = 1.f / sqrtf(q * (1.f / DM) + 1e-5f);
  u32x4 o = {pk2(f[0] * rs, f[1] * rs), pk2(f[2] * rs, f[3] * rs), pk2(f[4] * rs, f[5] * rs), pk2(f[6] * rs, f[7] * rs)};
  *(u32x4*)(ws + WS_HN + ((size_t)row * DM + lane * 8) * 2) = o;
}

DI float gelu_t(float x) {
  const float y = 0.7978845608028654f * (x + 0.044715f * x * x * x);
  return x * __builtin_amdgcn_rcpf(1.f + __builtin_amdgcn_exp2f(-2.f * 1.4426950408889634f * y));
}

__global__ void __launch_bounds__(256) ffn1_kernel(const bf16* __restrict__ Wg, const bf16* __restrict__ Wu, char* ws) {
  extern __shared__ __attribute__((aligned(16))) char lds[];
  const int bid = blockIdx.x, mt = bid & 15, nt = bid >> 4;
  const bf16* A = (const bf16*)(ws + WS_HN) + (size_t)mt * 128 * DM;
  const int t = threadIdx.x, lane = t & 63, w = t >> 6, wr = w >> 1, wc = w & 1, c = lane & 15, g = lane >> 4;
  f32x4 acc[4][4];
  const ptrdiff_t du = Wu - Wg;
  auto bcol = [&](int ch) { int f = nt * 64 + (ch >> 3) * 32 + (ch & 3) * 8; f = f < DF - 8 ? f : DF - 8; return (ptrdiff_t)f + (((ch >> 2) & 1) ? du : 0); };
  gemm_tile<true>(A, DM, Wg, DF, bcol, 0, DM / 64, DM - 1, lds, acc);
  bf16* act = (bf16*)(ws + WS_ACT);
  const int f0 = nt * 64 + wc * 32;
  if (f0 < DF) {
#pragma unroll
    for (int mi = 0; mi < 4; ++mi)
#pragma unroll
      for (int ni = 0; ni < 2; ++ni) {
        const int tok = mt * 128 + wr * 64 + mi * 16 + c, f = f0 + ni * 16 + 4 * g;
        float r[4];
#pragma unroll
        for (int j = 0; j < 4; ++j) r[j] = gelu_t(acc[mi][ni][j]) * acc[mi][ni + 2][j];
        u32x2 o = {pk2(r[0], r[1]), pk2(r[2], r[3])};
        *(u32x2*)(act + (size_t)tok * DFP + f) = o;
      }
  }
}

__global__ void __launch_bounds__(256) ffn2_kernel(const bf16* __restrict__ Wd, char* ws, bf16* __restrict__ out) {
  extern __shared__ __attribute__((aligned(16))) char lds[];
  const int bid = blockIdx.x, mt = bid & 15, nt = bid >> 4;
  const bf16* A = (const bf16*)(ws + WS_ACT) + (size_t)mt * 128 * DFP;
  const int t = threadIdx.x, lane = t & 63, w = t >> 6, wr = w >> 1, wc = w & 1, c = lane & 15, g = lane >> 4;
  f32x4 acc[4][4];
  auto bcol = [&](int ch) { return nt * 128 + ch * 8; };
  gemm_tile<true>(A, DFP, Wd, DM, bcol, 0, DFP / 64, DF - 1, lds, acc);
  const bf16* H = (const bf16*)(ws + WS_H);
#pragma unroll
  for (int mi = 0; mi < 4; ++mi)
#pragma unroll
    for (int ni = 0; ni < 4; ++ni) {
      const int tok = mt * 128 + wr * 64 + mi * 16 + c, n = nt * 128 + wc * 64 + ni * 16 + 4 * g;
      u32x2 hv = *(const u32x2*)(H + (size_t)tok * DM + n);
      unsigned d0 = pk2(acc[mi][ni][0], acc[mi][ni][1]), d1 = pk2(acc[mi][ni][2], acc[mi][ni][3]);
      u32x2 o = {pk2(bflo(hv[0]) + bflo(d0), bfhi(hv[0]) + bfhi(d0)), pk2(bflo(hv[1]) + bflo(d1), bfhi(hv[1]) + bfhi(d1))};
      *(u32x2*)(out + (size_t)tok * DM + n) = o;
    }
}

extern "C" void kernel_launch(void* const* d_in, const int* in_sizes, int n_in, void* d_out, int out_size, void* d_ws, size_t ws_size,
                              hipStream_t stream) {
  const bf16* x = (const bf16*)d_in[0];
  const bf16* mem0 = (const bf16*)d_in[1];
  const bf16* Wq = (const bf16*)d_in[2];
  const bf16* Wk = (const bf16*)d_in[3];
  const bf16* Wv = (const bf16*)d_in[4];
  const bf16* Wo = (const bf16*)d_in[5];
  const bf16* Wg = (const bf16*)d_in[8];
  const bf16* Wu = (const bf16*)d_in[9];
  const bf16* Wd = (const bf16*)d_in[10];
  char* ws = (char*)d_ws;
  bf16* out = (bf16*)d_out;
  (void)hipFuncSetAttribute((const void*)chain_kernel, hipFuncAttributeMaxDynamicSharedMemorySize, CL_END);
  prep_kernel<<<512, 256, 0, stream>>>(x, mem0, ws);
  qkv_kernel<<<16 * 96, 256, 65536, stream>>>(Wq, Wk, Wv, ws);
  chain_kernel<<<32, 256, CL_END, stream>>>(Wq, Wk, Wv, Wo, ws);
  attn_kernel<<<512, 256, 0, stream>>>(ws);
  wo_kernel<<<256, 256, 65536, stream>>>(Wo, ws);
  ln2_kernel<<<512, 256, 0, stream>>>(x, ws);
  ffn1_kernel<<<16 * 22, 256, 65536, stream>>>(Wg, Wu, ws);
  ffn2_kernel<<<64, 256, 65536, stream>>>(Wd, ws, out);
}
```

```cpp
#include <hip/hip_runtime.h>
#include <hip/hip_bf16.h>
#include <stdint.h>
#include <type_traits>

typedef __bf16 bf16;
typedef __bf16 bf16x2 __attribute__((ext_vector_type(2)));
typedef __bf16 bf16x4 __attribute__((ext_vector_type(4)));
typedef __bf16 bf16x8 __attribute__((ext_vector_type(8)));
typedef __bf16 bf16x16 __attribute__((ext_vector_type(16)));
typedef short s16x4 __attribute__((ext_vector_type(4)));
typedef float f32x2 __attribute__((ext_vector_type(2)));
typedef float f32x4 __attribute__((ext_vector_type(4)));
typedef unsigned u32x2 __attribute__((ext_vector_type(2)));
typedef unsigned u32x4 __attribute__((ext_vector_type(4)));
typedef unsigned u32x8 __attribute__((ext_vector_type(8)));
#define LAS __attribute__((address_space(3)))
#define DI __device__ __forceinline__

constexpr int S_ = 2048, DM = 512, NH = 32, DH = 128, HD = NH * DH, MM = 8, SEG = 32, NSEG = S_ / SEG, DF = 1376, DFP = 1408;

constexpr size_t WS_PART = 0;
constexpr size_t WS_MBUF = WS_PART + 2u * 524288u;
constexpr size_t WS_XN   = WS_MBUF + 64u * 8192u;
constexpr size_t WS_Q    = WS_XN + (size_t)S_ * DM * 2;
constexpr size_t WS_K    = WS_Q + (size_t)S_ * HD * 2;
constexpr size_t WS_VT   = WS_K + (size_t)S_ * HD * 2;
constexpr size_t WS_KM   = WS_VT + (size_t)S_ * HD * 2;
constexpr size_t WS_VMT  = WS_KM + (size_t)NSEG * MM * HD * 2;
constexpr size_t WS_O    = WS_VMT + (size_t)NSEG * MM * HD * 2;
constexpr size_t WS_P    = WS_O + (size_t)S_ * HD * 2;
constexpr size_t WS_H    = WS_P + (size_t)4 * S_ * DM * 4;
constexpr size_t WS_HN   = WS_H + (size_t)S_ * DM * 2;
constexpr size_t WS_ACT  = WS_HN + (size_t)S_ * DM * 2;
constexpr size_t WS_END  = WS_ACT + (size_t)S_ * DFP * 2;

DI unsigned pk2(float a, float b) { f32x2 f = {a, b}; return __builtin_bit_cast(unsigned, __builtin_convertvector(f, bf16x2)); }
DI float bflo(unsigned u) { return __uint_as_float(u << 16); }
DI float bfhi(unsigned u) { return __uint_as_float(u & 0xffff0000u); }
DI f32x4 mfma16(bf16x8 a, bf16x8 b, f32x4 c) { return __builtin_amdgcn_mfma_f32_16x16x32_bf16(a, b, c, 0, 0, 0); }
DI f32x4 smf(bf16x8 a, bf16x16 b, f32x4 c, int idx) { return __builtin_amdgcn_smfmac_f32_16x16x64_bf16(a, b, c, idx, 0, 0); }
DI bf16x8 mk8(unsigned a, unsigned b, unsigned c, unsigned d) { u32x4 u = {a, b, c, d}; return __builtin_bit_cast(bf16x8, u); }
DI u32x2 trread(const char* lds_ptr) {
  s16x4 r = __builtin_amdgcn_ds_read_tr16_b64_v4i16((LAS s16x4*)lds_ptr);
  return __builtin_bit_cast(u32x2, r);
}
DI float xmax32(float x) { auto r = __builtin_amdgcn_permlane32_swap(__float_as_uint(x), __float_as_uint(x), false, false); return fmaxf(__uint_as_float(r[0]), __uint_as_float(r[1])); }
DI float xmax16(float x) { auto r = __builtin_amdgcn_permlane16_swap(__float_as_uint(x), __float_as_uint(x), false, false); return fmaxf(__uint_as_float(r[0]), __uint_as_float(r[1])); }
DI float xsum32(float x) { auto r = __builtin_amdgcn_permlane32_swap(__float_as_uint(x), __float_as_uint(x), false, false); return __uint_as_float(r[0]) + __uint_as_float(r[1]); }
DI float xsum16(float x) { auto r = __builtin_amdgcn_permlane16_swap(__float_as_uint(x), __float_as_uint(x), false, false); return __uint_as_float(r[0]) + __uint_as_float(r[1]); }
DI float comb(float a, float b) { auto r = __builtin_amdgcn_permlane32_swap(__float_as_uint(a), __float_as_uint(b), false, false); return __uint_as_float(r[0]) + __uint_as_float(r[1]); }

__global__ void __launch_bounds__(256) prep_kernel(const bf16* __restrict__ x, const bf16* __restrict__ mem0, char* ws) {
  const int t = threadIdx.x, lane = t & 63, w = t >> 6;
  const int row = blockIdx.x * 4 + w;
  {
    u32x4 v = *(const u32x4*)(x + (size_t)row * DM + lane * 8);
    float f[8] = {bflo(v[0]), bfhi(v[0]), bflo(v[1]), bfhi(v[1]), bflo(v[2]), bfhi(v[2]), bflo(v[3]), bfhi(v[3])};
    float s = 0;
#pragma unroll
    for (int i = 0; i < 8; ++i) s += f[i];
#pragma unroll
    for (int m = 32; m >= 1; m >>= 1) s += __shfl_xor(s, m);
    const float mean = s * (1.f / DM);
    float q = 0;
#pragma unroll
    for (int i = 0; i < 8; ++i) { f[i] -= mean; q += f[i] * f[i]; }
#pragma unroll
    for (int m = 32; m >= 1; m >>= 1) q += __shfl_xor(q, m);
    const float rs = 1.f / sqrtf(q * (1.f / DM) + 1e-5f);
    u32x4 o = {pk2(f[0] * rs, f[1] * rs), pk2(f[2] * rs, f[3] * rs), pk2(f[4] * rs, f[5] * rs), pk2(f[6] * rs, f[7] * rs)};
    *(u32x4*)(ws + WS_XN + ((size_t)row * DM + lane * 8) * 2) = o;
  }
  const int gt = blockIdx.x * 256 + t;
  if (gt < 65536) { u32x4 z = {0, 0, 0, 0}; *(u32x4*)(ws + WS_PART + (size_t)gt * 16) = z; }
  else if (gt < 65536 + 32768) {
    const int u = gt - 65536;
    u32x4 z = {~0u, ~0u, ~0u, ~0u};
    if (u < 512) {
      const int ks = u >> 6, ga = (u >> 4) & 3, hb = (u >> 3) & 1, i = u & 7;
#pragma unroll
      for (int a = 0; a < 4; ++a) z[a] = *(const unsigned*)(mem0 + i * DM + ks * 64 + ga * 16 + 4 * a + 2 * hb);
#pragma unroll
      for (int j = 0; j < 4; ++j) if (z[j] == ~0u) z[j] = 0x7fff7fffu;
    }
    *(u32x4*)(ws + WS_MBUF + (size_t)u * 16) = z;
  }
  if (gt < S_ * 4) { const int r = gt >> 2, c = gt & 3; u32x4 z = {0, 0, 0, 0}; *(u32x4*)(ws + WS_ACT + ((size_t)r * DFP + DF + c * 8) * 2) = z; }
}

DI unsigned off_b(unsigned row, unsigned ch) { return 256u * row + 16u * (ch ^ (((row & 3) << 2) | ((row >> 2) & 3))); }

template <bool WA, class BCol>
DI void gemm_tile(const bf16* __restrict__ A, int lda, const bf16* __restrict__ B, int ldb, BCol bcol, int kbeg, int nkt, int kmaxB,
                  char* lds, f32x4 (&acc)[4][4]) {
  const int t = threadIdx.x, lane = t & 63, w = t >> 6, wr = w >> 1, wc = w & 1, c = lane & 15, g = lane >> 4;
  const bf16* asrc[4]; const bf16* bsrc[4]; int brow[4];
#pragma unroll
  for (int i = 0; i < 4; ++i) {
    const int u = i * 256 + t;
    const int r = u >> 3, sl = u & 7;
    asrc[i] = A + (size_t)r * lda + kbeg + ((sl ^ (r & 7)) << 3);
    const int kr = u >> 4, sb = u & 15;
    const int ch = sb ^ (((kr & 3) << 2) | ((kr >> 2) & 3));
    brow[i] = kbeg + kr;
    bsrc[i] = B + bcol(ch);
  }
  auto stage = [&](int kt, int buf) {
    char* la = lds + buf * 32768; char* lb = la + 16384;
#pragma unroll
    for (int i = 0; i < 4; ++i) {
      __builtin_amdgcn_global_load_lds((const unsigned*)(asrc[i] + kt * 64), (LAS unsigned*)(la + (i * 256 + w * 64) * 16), 16, 0, 0);
      int kr = brow[i] + kt * 64; kr = kr < kmaxB ? kr : kmaxB;
      __builtin_amdgcn_global_load_lds((const unsigned*)(bsrc[i] + (size_t)kr * ldb), (LAS unsigned*)(lb + (i * 256 + w * 64) * 16), 16, 0, 0);
    }
  };
#pragma unroll
  for (int i = 0; i < 4; ++i)
#pragma unroll
    for (int j = 0; j < 4; ++j) acc[i][j] = (f32x4){0, 0, 0, 0};
  stage(0, 0);
  __syncthreads();
  const int q = c >> 2, p = c & 3;
  for (int kt = 0; kt < nkt; ++kt) {
    const int buf = kt & 1;
    if (kt + 1 < nkt) stage(kt + 1, buf ^ 1);
    const char* la = lds + buf * 32768; const char* lb = la + 16384;
#pragma unroll
    for (int ks = 0; ks < 2; ++ks) {
      bf16x8 af[4], bfr[4];
#pragma unroll
      for (int mi = 0; mi < 4; ++mi) {
        const int r = wr * 64 + mi * 16 + c;
        af[mi] = *(const bf16x8*)(la + r * 128 + (((ks * 4 + g) ^ (r & 7)) << 4));
      }
#pragma unroll
      for (int ni = 0; ni < 4; ++ni) {
        const int cc = wc * 4 + ni;
        u32x2 lo = trread(lb + off_b(32 * ks + 8 * g + q, 2 * cc + (p >> 1)) + 8 * (p & 1));
        u32x2 hi = trread(lb + off_b(32 * ks + 8 * g + 4 + q, 2 * cc + (p >> 1)) + 8 * (p & 1));
        bfr[ni] = mk8(lo[0], lo[1], hi[0], hi[1]);
      }
#pragma unroll
      for (int mi = 0; mi < 4; ++mi)
#pragma unroll
        for (int ni = 0; ni < 4; ++ni)
          acc[mi][ni] = WA ? mfma16(bfr[ni], af[mi], acc[mi][ni]) : mfma16(af[mi], bfr[ni], acc[mi][ni]);
    }
    __syncthreads();
  }
}

__global__ void __launch_bounds__(256) qkv_kernel(const bf16* __restrict__ Wq, const bf16* __restrict__ Wk, const bf16* __restrict__ Wv, char* ws) {
  extern __shared__ __attribute__((aligned(16))) char lds[];
  const int bid = blockIdx.x;
  const int mt = bid & 15, nt = bid >> 4;
  const int mat = nt >> 5, h = nt & 31;
  const bf16* W = mat == 0 ? Wq : (mat == 1 ? Wk : Wv);
  const bf16* A = (const bf16*)(ws + WS_XN) + (size_t)mt * 128 * DM;
  const int t = threadIdx.x, lane = t & 63, w = t >> 6, wr = w >> 1, wc = w & 1, c = lane & 15, g = lane >> 4;
  f32x4 acc[4][4];
  auto bcol = [&](int ch) { return h * 128 + ch * 8; };
  if (mat < 2) {
    gemm_tile<true>(A, DM, W, HD, bcol, 0, DM / 64, DM - 1, lds, acc);
    bf16* O = (bf16*)(ws + (mat == 0 ? WS_Q : WS_K));
#pragma unroll
    for (int mi = 0; mi < 4; ++mi)
#pragma unroll
      for (int ni = 0; ni < 4; ++ni) {
        const int tok = mt * 128 + wr * 64 + mi * 16 + c, d = wc * 64 + ni * 16 + 4 * g;
        u32x2 o = {pk2(acc[mi][ni][0], acc[mi][ni][1]), pk2(acc[mi][ni][2], acc[mi][ni][3])};
        *(u32x2*)(O + (size_t)tok * HD + h * 128 + d) = o;
      }
  } else {
    gemm_tile<false>(A, DM, W, HD, bcol, 0, DM / 64, DM - 1, lds, acc);
    bf16* O = (bf16*)(ws + WS_VT);
#pragma unroll
    for (int mi = 0; mi < 4; ++mi)
#pragma unroll
      for (int ni = 0; ni < 4; ++ni) {
        const int tok0 = mt * 128 + wr * 64 + mi * 16;
        const int seg = tok0 >> 5, xt = (tok0 >> 4) & 1, d = wc * 64 + ni * 16 + c;
        u32x2 o = {pk2(acc[mi][ni][0], acc[mi][ni][1]), pk2(acc[mi][ni][2], acc[mi][ni][3])};
        *(u32x2*)(O + (((size_t)seg * NH + h) * DH + d) * 32 + g * 8 + xt * 4) = o;
      }
  }
}

constexpr int CL_W = 0, CL_M = 131072, CL_QK = CL_M + 8192, CL_KX = CL_QK + 4096, CL_O = CL_KX + 16384, CL_R = CL_O + 2048, CL_END = CL_R + 2048;
#define DLY1 8
#define DLY2 12
constexpr unsigned SPIN_CAP = 1u << 18;
#define ACLOB "a0","a1","a2","a3","a4","a5","a6","a7","a8","a9","a10","a11","a12","a13","a14","a15","a16","a17","a18","a19","a20","a21","a22","a23","a24","a25","a26","a27","a28","a29","a30","a31","a32","a33","a34","a35","a36","a37","a38","a39","a40","a41","a42","a43","a44","a45","a46","a47","a48","a49","a50","a51","a52","a53","a54","a55","a56","a57","a58","a59","a60","a61","a62","a63","a64","a65","a66","a67","a68","a69","a70","a71","a72","a73","a74","a75","a76","a77","a78","a79","a80","a81","a82","a83","a84","a85","a86","a87","a88","a89","a90","a91","a92","a93","a94","a95","a96","a97","a98","a99","a100","a101","a102","a103","a104","a105","a106","a107","a108","a109","a110","a111","a112","a113","a114","a115","a116","a117","a118","a119","a120","a121","a122","a123","a124","a125","a126","a127","a128","a129","a130","a131","a132","a133","a134","a135","a136","a137","a138","a139","a140","a141","a142","a143","a144","a145","a146","a147","a148","a149","a150","a151","a152","a153","a154","a155","a156","a157","a158","a159","a160","a161","a162","a163","a164","a165","a166","a167","a168","a169","a170","a171","a172","a173","a174","a175","a176","a177","a178","a179","a180","a181","a182","a183","a184","a185","a186","a187","a188","a189","a190","a191","a192","a193","a194","a195","a196","a197","a198","a199","a200","a201","a202","a203","a204","a205","a206","a207","a208","a209","a210","a211","a212","a213","a214","a215","a216","a217","a218","a219","a220","a221","a222","a223","a224","a225","a226","a227","a228","a229","a230","a231","a232","a233","a234","a235","a236","a237","a238","a239","a240","a241","a242","a243","a244","a245","a246","a247","a248","a249","a250","a251","a252","a253","a254","a255"

DI float max3f(float a, float b, float c) { float r; asm("v_max3_f32 %0, %1, %2, %3" : "=v"(r) : "v"(a), "v"(b), "v"(c)); return r; }
DI float max2f(float a, float b) { float r; asm("v_max_f32_e32 %0, %1, %2" : "=v"(r) : "v"(a), "v"(b)); return r; }
DI void st16_sc1(void* p, u32x4 v) { asm volatile("global_store_dwordx4 %0, %1, off sc1\n\ts_nop 1" ::"v"(p), "v"(v) : "memory"); }
DI void st4_sc1(void* p, unsigned v) { asm volatile("global_store_dword %0, %1, off sc1" ::"v"(p), "v"(v) : "memory"); }
DI void dma16(const void* src, unsigned dst) {
  asm volatile("s_mov_b32 m0, %1\n\ts_nop 0\n\tglobal_load_lds_dwordx4 %0, off" ::"v"(src), "s"(dst) : "memory", "m0");
}
template <int N, int OFF> DI void tr_to_acc(unsigned addr) {
  asm volatile("ds_read_b64_tr_b16 a[%c1:%c2], %0 offset:%c3" ::"v"(addr), "i"(N), "i"(N + 1), "i"(OFF) : "memory", ACLOB);
}
template <int A0, int BUF> DI void chunk_to_acc(unsigned addr) {
  tr_to_acc<A0 + 0, BUF + 0>(addr); tr_to_acc<A0 + 2, BUF + 256>(addr); tr_to_acc<A0 + 4, BUF + 2048>(addr); tr_to_acc<A0 + 6, BUF + 2304>(addr);
  tr_to_acc<A0 + 8, BUF + 32>(addr); tr_to_acc<A0 + 10, BUF + 288>(addr); tr_to_acc<A0 + 12, BUF + 2080>(addr); tr_to_acc<A0 + 14, BUF + 2336>(addr);
}
template <int KS> DI void qk_step(f32x4& q0, f32x4& q1, f32x4& k0, f32x4& k1, bf16x8 m, int sidx) {
  asm volatile("v_smfmac_f32_16x16x64_bf16 %0, %4, a[%c6:%c7], %5\n\t"
               "v_smfmac_f32_16x16x64_bf16 %1, %4, a[%c8:%c9], %5\n\t"
               "v_smfmac_f32_16x16x64_bf16 %2, %4, a[%c10:%c11], %5\n\t"
               "v_smfmac_f32_16x16x64_bf16 %3, %4, a[%c12:%c13], %5"
               : "+v"(q0), "+v"(q1), "+v"(k0), "+v"(k1)
               : "v"(m), "v"(sidx), "i"(KS * 32), "i"(KS * 32 + 7), "i"(KS * 32 + 8), "i"(KS * 32 + 15), "i"(KS * 32 + 16), "i"(KS * 32 + 23), "i"(KS * 32 + 24), "i"(KS * 32 + 31)
               : ACLOB);
}
DI void smf2(f32x4& d0, f32x4& d1, bf16x8 m, u32x8 w0, u32x8 w1, int sidx) {
  asm volatile("v_smfmac_f32_16x16x64_bf16 %0, %2, %3, %5\n\tv_smfmac_f32_16x16x64_bf16 %1, %2, %4, %5"
               : "+v"(d0), "+v"(d1) : "v"(m), "v"(w0), "v"(w1), "v"(sidx));
}
#define STAMP(i)

__global__ void __launch_bounds__(256, 1) chain_kernel(const bf16* __restrict__ Wq, const bf16* __restrict__ Wk, const bf16* __restrict__ Wv,
                                                       const bf16* __restrict__ Wo, char* ws) {
  extern __shared__ __attribute__((aligned(16))) char lds[];
  const int cu = blockIdx.x, h = cu >> 1, jh = cu & 1;
  const int t = threadIdx.x, lane = t & 63, w = __builtin_amdgcn_readfirstlane(t >> 6), c = lane & 15, g = lane >> 4;
  u32x8 wvr[4][2];
  const unsigned ldsbase = (unsigned)(size_t)(LAS char*)lds;
  char* const wl = lds + CL_W + w * 32768 + lane * 16;
  {
    const unsigned stg = ldsbase + CL_W + w * 32768;
    const size_t soff = (size_t)(lane >> 2) * HD + h * 128 + w * 32 + (lane & 3) * 8;
    auto issue = [&](const bf16* W, int ks, int buf) {
#pragma unroll
      for (int i = 0; i < 4; ++i) dma16(W + (size_t)(ks * 64 + i * 16) * HD + soff, stg + buf * 4096 + i * 1024);
    };
    const unsigned ta = stg + (8 * g + (c >> 2)) * 64 + (c & 3) * 8;
#define WAITV(n) asm volatile("s_waitcnt vmcnt(" #n ")" ::: "memory")
#define WAITL() asm volatile("s_waitcnt lgkmcnt(0)" ::: "memory")
    issue(Wq, 0, 0); issue(Wk, 0, 1); issue(Wq, 1, 2); issue(Wk, 1, 3); issue(Wq, 2, 4); issue(Wk, 2, 5); issue(Wq, 3, 6); issue(Wk, 3, 7);
    WAITV(24); chunk_to_acc<0, 0>(ta); chunk_to_acc<16, 4096>(ta); WAITL(); issue(Wq, 4, 0); issue(Wk, 4, 1);
    WAITV(24); chunk_to_acc<32, 8192>(ta); chunk_to_acc<48, 12288>(ta); WAITL(); issue(Wq, 5, 2); issue(Wk, 5, 3);
    WAITV(24); chunk_to_acc<64, 16384>(ta); chunk_to_acc<80, 20480>(ta); WAITL(); issue(Wq, 6, 4); issue(Wk, 6, 5);
    WAITV(24); chunk_to_acc<96, 24576>(ta); chunk_to_acc<112, 28672>(ta); WAITL(); issue(Wq, 7, 6); issue(Wk, 7, 7);
    WAITV(24); chunk_to_acc<128, 0>(ta); chunk_to_acc<144, 4096>(ta); WAITL(); issue(Wv, 0, 0); issue(Wv, 1, 1);
    WAITV(24); chunk_to_acc<160, 8192>(ta); chunk_to_acc<176, 12288>(ta); WAITL(); issue(Wv, 2, 2); issue(Wv, 3, 3);
    WAITV(24); chunk_to_acc<192, 16384>(ta); chunk_to_acc<208, 20480>(ta); WAITL(); issue(Wv, 4, 4); issue(Wv, 5, 5);
    WAITV(24); chunk_to_acc<224, 24576>(ta); chunk_to_acc<240, 28672>(ta); WAITL(); issue(Wv, 6, 6); issue(Wv, 7, 7);
    const char* tv = lds + CL_W + w * 32768 + (8 * g + (c >> 2)) * 64 + (c & 3) * 16;
    auto rdv = [&](int buf, u32x8 (&d)[2]) {
#pragma unroll
      for (int e = 0; e < 2; ++e)
#pragma unroll
        for (int j = 0; j < 4; ++j) { u32x2 r = trread(tv + buf * 4096 + (j >> 1) * 2048 + (j & 1) * 256 + e * 8); d[e][2 * j] = r[0]; d[e][2 * j + 1] = r[1]; }
    };
    WAITV(16);
#pragma unroll
    for (int ks = 0; ks < 4; ++ks) rdv(ks, wvr[ks]);
    u32x8 tmp[4][2];
    WAITV(0);
#pragma unroll
    for (int ks = 0; ks < 4; ++ks) rdv(4 + ks, tmp[ks]);
    asm volatile("s_waitcnt lgkmcnt(0)" : "+v"(tmp[0][0]), "+v"(tmp[0][1]), "+v"(tmp[1][0]), "+v"(tmp[1][1]), "+v"(tmp[2][0]), "+v"(tmp[2][1]), "+v"(tmp[3][0]), "+v"(tmp[3][1]),
                 "+v"(wvr[0][0]), "+v"(wvr[0][1]), "+v"(wvr[1][0]), "+v"(wvr[1][1]), "+v"(wvr[2][0]), "+v"(wvr[2][1]), "+v"(wvr[3][0]), "+v"(wvr[3][1]) :: "memory");
    {
      const bf16* src = Wo + (size_t)(h * 128 + (lane >> 3)) * DM + jh * 256 + w * 64 + (lane & 7) * 8;
#pragma unroll
      for (int i = 0; i < 16; ++i) dma16(src + (size_t)i * 8 * DM, stg + 16384 + i * 1024);
    }
#pragma unroll
    for (int ks = 0; ks < 4; ++ks)
#pragma unroll
      for (int e = 0; e < 2; ++e) {
        char* p0 = wl + ks * 4096 + e * 2048;
        *(u32x4*)p0 = (u32x4){tmp[ks][e][0], tmp[ks][e][1], tmp[ks][e][2], tmp[ks][e][3]};
        *(u32x4*)(p0 + 1024) = (u32x4){tmp[ks][e][4], tmp[ks][e][5], tmp[ks][e][6], tmp[ks][e][7]};
      }
    WAITV(0);
    const char* to = lds + CL_W + w * 32768 + 16384 + (8 * g + (c >> 2)) * 128 + (c & 3) * 32;
#pragma unroll
    for (int T = 0; T < 4; ++T)
#pragma unroll
      for (int ks = 0; ks < 2; ++ks)
#pragma unroll
        for (int j = 0; j < 4; ++j) { u32x2 r = trread(to + ks * 8192 + (j >> 1) * 4096 + (j & 1) * 512 + T * 8); tmp[T][ks][2 * j] = r[0]; tmp[T][ks][2 * j + 1] = r[1]; }
    asm volatile("s_waitcnt lgkmcnt(0)" : "+v"(tmp[0][0]), "+v"(tmp[0][1]), "+v"(tmp[1][0]), "+v"(tmp[1][1]), "+v"(tmp[2][0]), "+v"(tmp[2][1]), "+v"(tmp[3][0]), "+v"(tmp[3][1]) :: "memory");
#pragma unroll
    for (int T = 0; T < 4; ++T)
#pragma unroll
      for (int ks = 0; ks < 2; ++ks) {
        char* p0 = wl + 16384 + (T * 2 + ks) * 2048;
        *(u32x4*)p0 = (u32x4){tmp[T][ks][0], tmp[T][ks][1], tmp[T][ks][2], tmp[T][ks][3]};
        *(u32x4*)(p0 + 1024) = (u32x4){tmp[T][ks][4], tmp[T][ks][5], tmp[T][ks][6], tmp[T][ks][7]};
      }
  }
  const int sidx = (c & 8) ? 0xEEEE : 0x4444;
  const bf16* Kx = (const bf16*)(ws + WS_K) + (size_t)c * HD + h * 128 + w * 32 + 8 * g;
  const bf16* Vt = (const bf16*)(ws + WS_VT) + ((size_t)h * DH + w * 32 + 8 * (c >> 2) + (c & 3)) * 32 + 8 * g;
  const float sc = 0.08838834764831845f * 1.4426950408889634f;
  u32x8 wr1[8];
  auto dmakv = [&](int sn) {
    const bf16* kp = Kx + (size_t)sn * 32 * HD; const bf16* vp = Vt + (size_t)sn * NH * DH * 32;
    const unsigned d0 = ldsbase + CL_KX + w * 1024;
    unsigned keep;
    asm volatile("s_mov_b32 %0, m0\n\t"
                 "s_mov_b32 m0, %4\n\ts_nop 0\n\tglobal_load_lds_dwordx4 %1, off\n\t"
                 "s_add_u32 m0, m0, 4096\n\ts_nop 0\n\tglobal_load_lds_dwordx4 %2, off\n\t"
                 "s_add_u32 m0, m0, 4096\n\ts_nop 0\n\tglobal_load_lds_dwordx4 %3, off\n\t"
                 "s_add_u32 m0, m0, 3840\n\ts_nop 0\n\tglobal_load_lds_dwordx4 %3, off offset:256\n\t"
                 "s_mov_b32 m0, %0"
                 : "=&s"(keep) : "v"(kp), "v"(kp + (size_t)16 * HD), "v"(vp), "s"(d0) : "memory", "scc");
  };
  auto ldw = [&](int off) {
#pragma unroll
    for (int i = 0; i < 8; ++i) {
      u32x4 a = *(const u32x4*)(wl + off + i * 2048), b = *(const u32x4*)(wl + off + i * 2048 + 1024);
      wr1[i] = (u32x8){a[0], a[1], a[2], a[3], b[0], b[1], b[2], b[3]};
    }
  };
  dmakv(0);
  ldw(0);
  unsigned cap = SPIN_CAP;
  const unsigned ones2 = 0x3f803f80u;
  const bf16x8 onesx = mk8(ones2, ones2, ones2, ones2);
  const bf16x8 onesm = mk8(g < 2 ? ones2 : 0u, g < 2 ? ones2 : 0u, 0u, 0u);
  const unsigned tmask = ~127u;
  char* const pdst0 = ws + WS_PART + ((size_t)(32 * jh + 8 * w + (c >> 1)) * 32 + h) * 256 + ((c & 1) * 4 + (lane >> 5) * 2 + (g & 1)) * 16;
  char* const psrc0 = ws + WS_PART + (size_t)cu * 8192 + t * 16;
  auto step = [&](const int s, auto LASTC) {
    constexpr bool LAST = decltype(LASTC)::value;
    STAMP(0);
    {
      const char* src = ws + WS_MBUF + (size_t)s * 8192 + t * 16;
      u32x4 a, b;
      for (unsigned spins = 0;; ++spins) {
        asm volatile("global_load_dwordx4 %0, %2, off sc1\n\tglobal_load_dwordx4 %1, %3, off sc1\n\ts_waitcnt vmcnt(0)"
                     : "=&v"(a), "=&v"(b) : "v"(src), "v"(src + 4096) : "memory");
        const unsigned mxu = max(max(max(a[0], a[1]), max(a[2], a[3])), max(max(b[0], b[1]), max(b[2], b[3])));
        if (__all(mxu != ~0u)) break;
        if (spins >= cap) { cap = 0; break; }
      }
      *(u32x4*)(lds + CL_M + t * 16) = a;
      *(u32x4*)(lds + CL_M + 4096 + t * 16) = b;
    }
    STAMP(1);
    __syncthreads();
    f32x4 aq[2], ak[2], av[2];
#pragma unroll
    for (int i = 0; i < 2; ++i) { aq[i] = (f32x4){0, 0, 0, 0}; ak[i] = aq[i]; av[i] = aq[i]; }
    bf16x8 mf[8];
#pragma unroll
    for (int ks = 0; ks < 8; ++ks) mf[ks] = *(const bf16x8*)(lds + CL_M + ks * 1024 + lane * 16);
    asm volatile("s_nop 1" : "+v"(aq[0]), "+v"(aq[1]), "+v"(ak[0]), "+v"(ak[1]), "+v"(av[0]), "+v"(av[1]));
    qk_step<0>(aq[0], aq[1], ak[0], ak[1], mf[0], sidx); qk_step<1>(aq[0], aq[1], ak[0], ak[1], mf[1], sidx);
    qk_step<2>(aq[0], aq[1], ak[0], ak[1], mf[2], sidx); qk_step<3>(aq[0], aq[1], ak[0], ak[1], mf[3], sidx);
    qk_step<4>(aq[0], aq[1], ak[0], ak[1], mf[4], sidx); qk_step<5>(aq[0], aq[1], ak[0], ak[1], mf[5], sidx);
    qk_step<6>(aq[0], aq[1], ak[0], ak[1], mf[6], sidx); qk_step<7>(aq[0], aq[1], ak[0], ak[1], mf[7], sidx);
    smf2(av[0], av[1], mf[4], wr1[0], wr1[1], sidx);
    asm volatile("s_nop 7" : "+v"(aq[0]), "+v"(aq[1]), "+v"(ak[0]), "+v"(ak[1]));
    STAMP(2);
    {
      const int off = (w * 32 + 16 * (lane >> 5) + c) * 16 + (g & 1) * 8;
      u32x2 qq = {pk2(comb(aq[0][0], aq[1][0]), comb(aq[0][1], aq[1][1])), pk2(comb(aq[0][2], aq[1][2]), comb(aq[0][3], aq[1][3]))};
      smf2(av[0], av[1], mf[5], wr1[2], wr1[3], sidx);
      u32x2 kk = {pk2(comb(ak[0][0], ak[1][0]), comb(ak[0][1], ak[1][1])), pk2(comb(ak[0][2], ak[1][2]), comb(ak[0][3], ak[1][3]))};
      *(u32x2*)(lds + CL_QK + off) = qq; *(u32x2*)(lds + CL_QK + 2048 + off) = kk;
    }
    smf2(av[0], av[1], mf[6], wr1[4], wr1[5], sidx);
    smf2(av[0], av[1], mf[7], wr1[6], wr1[7], sidx);
    bf16x8 kxf[2][4], vx[2];
#pragma unroll
    for (int x = 0; x < 2; ++x) {
#pragma unroll
      for (int ks = 0; ks < 4; ++ks) kxf[x][ks] = *(const bf16x8*)(lds + CL_KX + (x * 4 + ks) * 1024 + lane * 16);
      vx[x] = *(const bf16x8*)(lds + CL_KX + 8192 + (x * 4 + w) * 1024 + lane * 16);
    }
    STAMP(3);
    __syncthreads();
    STAMP(4);
    bf16x8 qf[4], kf[4];
    {
      const int off = (8 * g + (c >> 2)) * 16 + (c & 1) * 8;
#pragma unroll
      for (int ks = 0; ks < 4; ++ks) {
        u32x2 a = trread(lds + CL_QK + ks * 512 + off), b = trread(lds + CL_QK + ks * 512 + off + 64);
        qf[ks] = mk8(a[0], a[1], b[0], b[1]);
        a = trread(lds + CL_QK + 2048 + ks * 512 + off); b = trread(lds + CL_QK + 2048 + ks * 512 + off + 64);
        kf[ks] = mk8(a[0], a[1], b[0], b[1]);
      }
    }
    smf2(av[0], av[1], mf[0], wvr[0][0], wvr[0][1], sidx);
    smf2(av[0], av[1], mf[1], wvr[1][0], wvr[1][1], sidx);
    if constexpr (!LAST) dmakv(s + 1);
    smf2(av[0], av[1], mf[2], wvr[2][0], wvr[2][1], sidx);
    smf2(av[0], av[1], mf[3], wvr[3][0], wvr[3][1], sidx);
    auto publish_kv = [&]() {
      if (jh == 0) {
        if (w == 0 && c < 8) {
          bf16* kd = (bf16*)(ws + WS_KM) + (size_t)(s * 8 + c) * HD + h * 128 + 8 * g;
#pragma unroll
          for (int ks = 0; ks < 4; ++ks) *(bf16x8*)(kd + ks * 32) = kf[ks];
        }
      } else {
        u32x2 vv = {pk2(comb(av[0][0], av[1][0]), comb(av[0][1], av[1][1])), pk2(comb(av[0][2], av[1][2]), comb(av[0][3], av[1][3]))};
        *(u32x2*)((bf16*)(ws + WS_VMT) + (((size_t)s * NH + h) * DH + w * 32 + 8 * (c >> 2) + 4 * (lane >> 5) + (c & 3)) * 8 + (g & 1) * 4) = vv;
      }
    };
    if constexpr (LAST) { asm volatile("s_nop 7" : "+v"(av[0]), "+v"(av[1])); publish_kv(); return; }
    f32x4 sm[3];
    asm volatile("s_nop 1\n\tv_mfma_f32_16x16x32_bf16 %0, %3, %6, 0\n\tv_mfma_f32_16x16x32_bf16 %1, %4, %6, 0\n\tv_mfma_f32_16x16x32_bf16 %2, %5, %6, 0"
                 : "=&v"(sm[0]), "=&v"(sm[1]), "=&v"(sm[2]) : "v"(kf[0]), "v"(kxf[0][0]), "v"(kxf[1][0]), "v"(qf[0]));
#pragma unroll
    for (int ks = 1; ks < 4; ++ks)
      asm volatile("s_nop 1\n\tv_mfma_f32_16x16x32_bf16 %0, %3, %6, %0\n\tv_mfma_f32_16x16x32_bf16 %1, %4, %6, %1\n\tv_mfma_f32_16x16x32_bf16 %2, %5, %6, %2"
                   : "+v"(sm[0]), "+v"(sm[1]), "+v"(sm[2]) : "v"(kf[ks]), "v"(kxf[0][ks]), "v"(kxf[1][ks]), "v"(qf[ks]));
    asm volatile("" : "+v"(av[0]), "+v"(av[1]));
    const bf16x8 vm0 = mk8(pk2(av[0][0], av[0][1]), pk2(av[0][2], av[0][3]), 0u, 0u);
    const bf16x8 vm1 = mk8(pk2(av[1][0], av[1][1]), pk2(av[1][2], av[1][3]), 0u, 0u);
    ldw(16384);
    asm volatile("s_nop 7" : "+v"(sm[0]), "+v"(sm[1]), "+v"(sm[2]));
    STAMP(10);
    float mx = max3f(max3f(sm[0][0], sm[0][1], sm[0][2]), max3f(sm[0][3], sm[1][0], sm[1][1]), max3f(max3f(sm[1][2], sm[1][3], sm[2][0]), max3f(sm[2][1], sm[2][2], sm[2][3]), sm[2][3]));
    {
      auto r = __builtin_amdgcn_permlane16_swap(__float_as_uint(mx), __float_as_uint(mx), false, false);
      mx = max2f(__uint_as_float(r[0]), __uint_as_float(r[1]));
      r = __builtin_amdgcn_permlane32_swap(__float_as_uint(mx), __float_as_uint(mx), false, false);
      mx = max2f(__uint_as_float(r[0]), __uint_as_float(r[1]));
    }
    const float nm = -mx * sc;
#pragma unroll
    for (int j = 0; j < 3; ++j)
#pragma unroll
      for (int r = 0; r < 4; ++r) sm[j][r] = __builtin_amdgcn_exp2f(__builtin_fmaf(sm[j][r], sc, nm));
    const bf16x8 px = mk8(pk2(sm[1][0], sm[1][1]), pk2(sm[1][2], sm[1][3]), pk2(sm[2][0], sm[2][1]), pk2(sm[2][2], sm[2][3]));
    const bf16x8 pm = mk8(pk2(sm[0][0], sm[0][1]), pk2(sm[0][2], sm[0][3]), 0u, 0u);
    STAMP(5);
    {
      f32x4 o0, o1, rs;
      asm volatile("s_nop 1\n\tv_mfma_f32_16x16x32_bf16 %2, %9, %7, 0\n\tv_mfma_f32_16x16x32_bf16 %0, %3, %7, 0\n\tv_mfma_f32_16x16x32_bf16 %2, %10, %8, %2\n\t"
                   "v_mfma_f32_16x16x32_bf16 %1, %4, %7, 0\n\tv_mfma_f32_16x16x32_bf16 %0, %5, %8, %0\n\tv_mfma_f32_16x16x32_bf16 %1, %6, %8, %1\n\ts_nop 4"
                   : "=&v"(o0), "=&v"(o1), "=&v"(rs) : "v"(vx[0]), "v"(vx[1]), "v"(vm0), "v"(vm1), "v"(px), "v"(pm), "v"(onesx), "v"(onesm));
      float inv = __builtin_amdgcn_rcpf(rs[0]);
      asm volatile("s_nop 2" : "+v"(o0), "+v"(o1), "+v"(inv));
      const bool hb = c >= 8;
      const float e0 = hb ? o0[2] : o0[0], e1 = hb ? o0[3] : o0[1], e2 = hb ? o1[2] : o1[0], e3 = hb ? o1[3] : o1[1];
      u32x2 o = {pk2(e0 * inv, e1 * inv), pk2(e2 * inv, e3 * inv)};
      *(u32x2*)(lds + CL_O + (w >> 1) * 1024 + (16 * (2 * (w & 1) + (g >> 1)) + c) * 16 + (g & 1) * 8) = o;
    }
    __syncthreads();
    STAMP(6);
    const size_t slot = (size_t)(s & 1) * 524288;
    {
      const bf16x8 of0 = *(const bf16x8*)(lds + CL_O + lane * 16), of1 = *(const bf16x8*)(lds + CL_O + 1024 + lane * 16);
      f32x4 d[4];
#pragma unroll
      for (int T = 0; T < 4; ++T) d[T] = (f32x4){0, 0, 0, 0};
      char* dst = pdst0 + slot;
      asm volatile("s_nop 1" : "+v"(d[0]), "+v"(d[1]), "+v"(d[2]), "+v"(d[3]), "+v"(dst));
      smf2(d[0], d[1], of0, wr1[0], wr1[2], sidx); smf2(d[0], d[1], of1, wr1[1], wr1[3], sidx);
      smf2(d[2], d[3], of0, wr1[4], wr1[6], sidx); smf2(d[2], d[3], of1, wr1[5], wr1[7], sidx);
      asm volatile("s_nop 4" : "+v"(d[0]), "+v"(d[1]));
      const unsigned tag = (unsigned)(s + 1);
#pragma unroll
      for (int tp = 0; tp < 2; ++tp) {
        u32x4 v;
#pragma unroll
        for (int r = 0; r < 4; ++r) asm("v_and_or_b32 %0, %1, %2, %3" : "=v"(v[r]) : "v"(comb(d[2 * tp][r], d[2 * tp + 1][r])), "v"(tmask), "s"(tag));
        if (tp == 0) { asm volatile("global_store_dwordx4 %0, %1, off sc1\n\ts_nop 1" ::"v"(dst), "v"(v) : "memory"); asm volatile("" : "+v"(d[2]), "+v"(d[3])); }
        else asm volatile("global_store_dwordx4 %0, %1, off offset:128 sc1\n\ts_nop 1" ::"v"(dst), "v"(v) : "memory");
      }
    }
    STAMP(7);
    publish_kv();
    ldw(0);
#pragma unroll 1
    for (int k = 0; k < DLY1; ++k) __builtin_amdgcn_s_sleep(1);
    STAMP(11);
    {
      char* src = psrc0 + slot;
      u32x4 r0, r1;
      const unsigned tag = (unsigned)(s + 1);
      for (unsigned spins = 0;; ++spins) {
        asm volatile("global_load_dwordx4 %0, %2, off sc1\n\tglobal_load_dwordx4 %1, %3, off sc1\n\ts_waitcnt vmcnt(0)"
                     : "=&v"(r0), "=&v"(r1) : "v"(src), "v"(src + 4096) : "memory");
        unsigned bad = 0;
#pragma unroll
        for (int j = 0; j < 4; ++j) bad |= (r0[j] ^ tag) | (r1[j] ^ tag);
        if (__all((bad & 127u) == 0)) break;
        if (spins >= cap) { cap = 0; break; }
      }
      STAMP(8);
      float x0 = __uint_as_float(r0[0]) + __uint_as_float(r1[0]), x1 = __uint_as_float(r0[1]) + __uint_as_float(r1[1]);
      float x2 = __uint_as_float(r0[2]) + __uint_as_float(r1[2]), x3 = __uint_as_float(r0[3]) + __uint_as_float(r1[3]);
      const float y0 = comb(x0, x2), y1 = comb(x1, x3);
      auto rr = __builtin_amdgcn_permlane16_swap(__float_as_uint(y0), __float_as_uint(y1), false, false);
      const float z = __uint_as_float(rr[0]) + __uint_as_float(rr[1]);
      *(float*)(lds + CL_R + w * 256 + c * 16 + g * 4) = z;
      __syncthreads();
      if (t < 32) {
        const int i = t >> 2, cp = t & 3;
        const float* R = (const float*)(lds + CL_R) + ((cp >> 1) * 8 + (cp & 1) * 2 + (i >> 2)) * 4 + (i & 3);
        const float a = (R[0] + R[64]) + (R[128] + R[192]);
        const float b = (R[16] + R[80]) + (R[144] + R[208]);
        unsigned o = pk2(a, b);
        if (o == ~0u) o = 0x7fff7fffu;
        st4_sc1(ws + WS_MBUF + (size_t)(s + 1) * 8192 + cu * 128 + t * 4, o);
      }
      STAMP(9);
      const u32x4 zz = {0, 0, 0, 0};
      st16_sc1(src, zz); st16_sc1(src + 4096, zz);
    }
#pragma unroll 1
    for (int k = 0; k < DLY2; ++k) __builtin_amdgcn_s_sleep(1);
  };
#pragma unroll 1
  for (int s = 0; s < NSEG - 1; ++s) step(s, std::false_type{});
  step(NSEG - 1, std::true_type{});
}

__global__ void __launch_bounds__(256) attn_kernel(char* ws) {
  const int t = threadIdx.x, lane = t & 63, w = t >> 6, c = lane & 15, g = lane >> 4;
  const int id = blockIdx.x * 4 + w, s = id >> 5, h = id & 31;
  const bf16* Q = (const bf16*)(ws + WS_Q); const bf16* K = (const bf16*)(ws + WS_K); const bf16* Vt = (const bf16*)(ws + WS_VT);
  const bf16* km = (const bf16*)(ws + WS_KM); const bf16* vmT = (const bf16*)(ws + WS_VMT);
  bf16* O = (bf16*)(ws + WS_O);
  f32x4 st[3][2];
#pragma unroll
  for (int j = 0; j < 3; ++j) { st[j][0] = (f32x4){0, 0, 0, 0}; st[j][1] = st[j][0]; }
#pragma unroll
  for (int ks = 0; ks < 4; ++ks) {
    const int col = h * 128 + ks * 32 + 8 * g;
    bf16x8 q0 = *(const bf16x8*)(Q + (size_t)(s * 32 + c) * HD + col), q1 = *(const bf16x8*)(Q + (size_t)(s * 32 + 16 + c) * HD + col);
    bf16x8 k0 = *(const bf16x8*)(K + (size_t)(s * 32 + c) * HD + col), k1 = *(const bf16x8*)(K + (size_t)(s * 32 + 16 + c) * HD + col);
    bf16x8 kmf = *(const bf16x8*)(km + (size_t)(s * 8 + (c & 7)) * HD + col);
    st[0][0] = mfma16(kmf, q0, st[0][0]); st[0][1] = mfma16(kmf, q1, st[0][1]);
    st[1][0] = mfma16(k0, q0, st[1][0]); st[1][1] = mfma16(k0, q1, st[1][1]);
    st[2][1] = mfma16(k1, q1, st[2][1]);
  }
  const float sc = 0.08838834764831845f * 1.4426950408889634f;
  bf16x8 px[2], pm[2];
#pragma unroll
  for (int qt = 0; qt < 2; ++qt) {
    float v[12];
#pragma unroll
    for (int r = 0; r < 4; ++r) {
      v[r] = (g < 2) ? st[0][qt][r] : -INFINITY;
      const int kj = 4 * g + r;
      v[4 + r] = (qt == 1 || kj <= c) ? st[1][qt][r] : -INFINITY;
      v[8 + r] = (qt == 1 && kj <= c) ? st[2][qt][r] : -INFINITY;
    }
    float mx = v[0];
#pragma unroll
    for (int i = 1; i < 12; ++i) mx = fmaxf(mx, v[i]);
    mx = xmax16(mx); mx = xmax32(mx);
    float sum = 0;
#pragma unroll
    for (int i = 0; i < 12; ++i) { v[i] = __builtin_amdgcn_exp2f((v[i] - mx) * sc); sum += v[i]; }
    sum = xsum16(sum); sum = xsum32(sum);
    const float inv = __builtin_amdgcn_rcpf(sum);
    px[qt] = mk8(pk2(v[4] * inv, v[5] * inv), pk2(v[6] * inv, v[7] * inv), pk2(v[8] * inv, v[9] * inv), pk2(v[10] * inv, v[11] * inv));
    pm[qt] = mk8(pk2(v[0] * inv, v[1] * inv), pk2(v[2] * inv, v[3] * inv), 0u, 0u);
  }
#pragma unroll
  for (int dt = 0; dt < 8; ++dt) {
    const size_t drow = ((size_t)s * NH + h) * DH + dt * 16 + c;
    bf16x8 vx = *(const bf16x8*)(Vt + drow * 32 + 8 * g);
    u32x2 vmr = *(const u32x2*)(vmT + drow * 8 + (g & 1) * 4);
    bf16x8 vm = mk8(vmr[0], vmr[1], 0u, 0u);
#pragma unroll
    for (int qt = 0; qt < 2; ++qt) {
      f32x4 o = mfma16(vx, px[qt], (f32x4){0, 0, 0, 0});
      o = mfma16(vm, pm[qt], o);
      u32x2 ov = {pk2(o[0], o[1]), pk2(o[2], o[3])};
      *(u32x2*)(O + (size_t)(s * 32 + qt * 16 + c) * HD + h * 128 + dt * 16 + 4 * g) = ov;
    }
  }
}

__global__ void __launch_bounds__(256) wo_kernel(const bf16* __restrict__ Wo, char* ws) {
  extern __shared__ __attribute__((aligned(16))) char lds[];
  const int bid = blockIdx.x, mt = bid & 15, nt = (bid >> 4) & 3, sk = bid >> 6;
  const bf16* A = (const bf16*)(ws + WS_O) + (size_t)mt * 128 * HD;
  const int t = threadIdx.x, lane = t & 63, w = t >> 6, wr = w >> 1, wc = w & 1, c = lane & 15, g = lane >> 4;
  f32x4 acc[4][4];
  auto bcol = [&](int ch) { return nt * 128 + ch * 8; };
  gemm_tile<true>(A, HD, Wo, DM, bcol, sk * 1024, 16, HD - 1, lds, acc);
  float* P = (float*)(ws + WS_P) + (size_t)sk * S_ * DM;
#pragma unroll
  for (int mi = 0; mi < 4; ++mi)
#pragma unroll
    for (int ni = 0; ni < 4; ++ni) {
      const int tok = mt * 128 + wr * 64 + mi * 16 + c, n = nt * 128 + wc * 64 + ni * 16 + 4 * g;
      *(f32x4*)(P + (size_t)tok * DM + n) = acc[mi][ni];
    }
}

__global__ void __launch_bounds__(256) ln2_kernel(const bf16* __restrict__ x, char* ws) {
  const int t = threadIdx.x, lane = t & 63, w = t >> 6;
  const int row = blockIdx.x * 4 + w;
  const float* P = (const float*)(ws + WS_P) + (size_t)row * DM + lane * 8;
  float a[8];
#pragma unroll
  for (int i = 0; i < 8; ++i) a[i] = 0;
#pragma unroll
  for (int k = 0; k < 4; ++k) {
    f32x4 p0 = *(const f32x4*)(P + (size_t)k * S_ * DM), p1 = *(const f32x4*)(P + (size_t)k * S_ * DM + 4);
#pragma unroll
    for (int i = 0; i < 4; ++i) { a[i] += p0[i]; a[4 + i] += p1[i]; }
  }
  u32x4 v = *(const u32x4*)(x + (size_t)row * DM + lane * 8);
  u32x4 hb;
#pragma unroll
  for (int i = 0; i < 4; ++i) {
    unsigned ar = pk2(a[2 * i], a[2 * i + 1]);
    hb[i] = pk2(bflo(v[i]) + bflo(ar), bfhi(v[i]) + bfhi(ar));
  }
  *(u32x4*)(ws + WS_H + ((size_t)row * DM + lane * 8) * 2) = hb;
  float f[8] = {bflo(hb[0]), bfhi(hb[0]), bflo(hb[1]), bfhi(hb[1]), bflo(hb[2]), bfhi(hb[2]), bflo(hb[3]), bfhi(hb[3])};
  float s = 0;
#pragma unroll
  for (int i = 0; i < 8; ++i) s += f[i];
#pragma unroll
  for (int m = 32; m >= 1; m >>= 1) s += __shfl_xor(s, m);
  const float mean = s * (1.f / DM);
  float q = 0;
#pragma unroll
  for (int i = 0; i < 8; ++i) { f[i] -= mean; q += f[i] * f[i]; }
#pragma unroll
  for (int m = 32; m >= 1; m >>= 1) q += __shfl_xor(q, m);
  const float rs = 1.f / sqrtf(q * (1.f / DM) + 1e-5f);
  u32x4 o = {pk2(f[0] * rs, f[1] * rs), pk2(f[2] * rs, f[3] * rs), pk2(f[4] * rs, f[5] * rs), pk2(f[6] * rs, f[7] * rs)};
  *(u32x4*)(ws + WS_HN + ((size_t)row * DM + lane * 8) * 2) = o;
}

DI float gelu_t(float x) {
  const float y = 0.7978845608028654f * (x + 0.044715f * x * x * x);
  return x * __builtin_amdgcn_rcpf(1.f + __builtin_amdgcn_exp2f(-2.f * 1.4426950408889634f * y));
}

__global__ void __launch_bounds__(256) ffn1_kernel(const bf16* __restrict__ Wg, const bf16* __restrict__ Wu, char* ws) {
  extern __shared__ __attribute__((aligned(16))) char lds[];
  const int bid = blockIdx.x, mt = bid & 15, nt = bid >> 4;
  const bf16* A = (const bf16*)(ws + WS_HN) + (size_t)mt * 128 * DM;
  const int t = threadIdx.x, lane = t & 63, w = t >> 6, wr = w >> 1, wc = w & 1, c = lane & 15, g = lane >> 4;
  f32x4 acc[4][4];
  const ptrdiff_t du = Wu - Wg;
  auto bcol = [&](int ch) { int f = nt * 64 + (ch >> 3) * 32 + (ch & 3) * 8; f = f < DF - 8 ? f : DF - 8; return (ptrdiff_t)f + (((ch >> 2) & 1) ? du : 0); };
  gemm_tile<true>(A, DM, Wg, DF, bcol, 0, DM / 64, DM - 1, lds, acc);
  bf16* act = (bf16*)(ws + WS_ACT);
  const int f0 = nt * 64 + wc * 32;
  if (f0 < DF) {
#pragma unroll
    for (int mi = 0; mi < 4; ++mi)
#pragma unroll
      for (int ni = 0; ni < 2; ++ni) {
        const int tok = mt * 128 + wr * 64 + mi * 16 + c, f = f0 + ni * 16 + 4 * g;
        float r[4];
#pragma unroll
        for (int j = 0; j < 4; ++j) r[j] = gelu_t(acc[mi][ni][j]) * acc[mi][ni + 2][j];
        u32x2 o = {pk2(r[0], r[1]), pk2(r[2], r[3])};
        *(u32x2*)(act + (size_t)tok * DFP + f) = o;
      }
  }
}

__global__ void __launch_bounds__(256) ffn2_kernel(const bf16* __restrict__ Wd, char* ws, bf16* __restrict__ out) {
  extern __shared__ __attribute__((aligned(16))) char lds[];
  const int bid = blockIdx.x, mt = bid & 15, nt = bid >> 4;
  const bf16* A = (const bf16*)(ws + WS_ACT) + (size_t)mt * 128 * DFP;
  const int t = threadIdx.x, lane = t & 63, w = t >> 6, wr = w >> 1, wc = w & 1, c = lane & 15, g = lane >> 4;
  f32x4 acc[4][4];
  auto bcol = [&](int ch) { return nt * 128 + ch * 8; };
  gemm_tile<true>(A, DFP, Wd, DM, bcol, 0, DFP / 64, DF - 1, lds, acc);
  const bf16* H = (const bf16*)(ws + WS_H);
#pragma unroll
  for (int mi = 0; mi < 4; ++mi)
#pragma unroll
    for (int ni = 0; ni < 4; ++ni) {
      const int tok = mt * 128 + wr * 64 + mi * 16 + c, n = nt * 128 + wc * 64 + ni * 16 + 4 * g;
      u32x2 hv = *(const u32x2*)(H + (size_t)tok * DM + n);
      unsigned d0 = pk2(acc[mi][ni][0], acc[mi][ni][1]), d1 = pk2(acc[mi][ni][2], acc[mi][ni][3]);
      u32x2 o = {pk2(bflo(hv[0]) + bflo(d0), bfhi(hv[0]) + bfhi(d0)), pk2(bflo(hv[1]) + bflo(d1), bfhi(hv[1]) + bfhi(d1))};
      *(u32x2*)(out + (size_t)tok * DM + n) = o;
    }
}

extern "C" void kernel_launch(void* const* d_in, const int* in_sizes, int n_in, void* d_out, int out_size, void* d_ws, size_t ws_size,
                              hipStream_t stream) {
  const bf16* x = (const bf16*)d_in[0];
  const bf16* mem0 = (const bf16*)d_in[1];
  const bf16* Wq = (const bf16*)d_in[2];
  const bf16* Wk = (const bf16*)d_in[3];
  const bf16* Wv = (const bf16*)d_in[4];
  const bf16* Wo = (const bf16*)d_in[5];
  const bf16* Wg = (const bf16*)d_in[8];
  const bf16* Wu = (const bf16*)d_in[9];
  const bf16* Wd = (const bf16*)d_in[10];
  char* ws = (char*)d_ws;
  bf16* out = (bf16*)d_out;
  (void)hipFuncSetAttribute((const void*)chain_kernel, hipFuncAttributeMaxDynamicSharedMemorySize, CL_END);
  prep_kernel<<<512, 256, 0, stream>>>(x, mem0, ws);
  qkv_kernel<<<16 * 96, 256, 65536, stream>>>(Wq, Wk, Wv, ws);
  chain_kernel<<<64, 256, CL_END, stream>>>(Wq, Wk, Wv, Wo, ws);
  attn_kernel<<<512, 256, 0, stream>>>(ws);
  wo_kernel<<<256, 256, 65536, stream>>>(Wo, ws);
  ln2_kernel<<<512, 256, 0, stream>>>(x, ws);
  ffn1_kernel<<<16 * 22, 256, 65536, stream>>>(Wg, Wu, ws);
  ffn2_kernel<<<64, 256, 65536, stream>>>(Wd, ws, out);
}
```

```cpp
#include <hip/hip_runtime.h>
#include <hip/hip_bf16.h>
#include <stdint.h>
#include <type_traits>

typedef __bf16 bf16;
typedef __bf16 bf16x2 __attribute__((ext_vector_type(2)));
typedef __bf16 bf16x4 __attribute__((ext_vector_type(4)));
typedef __bf16 bf16x8 __attribute__((ext_vector_type(8)));
typedef __bf16 bf16x16 __attribute__((ext_vector_type(16)));
typedef short s16x4 __attribute__((ext_vector_type(4)));
typedef float f32x2 __attribute__((ext_vector_type(2)));
typedef float f32x4 __attribute__((ext_vector_type(4)));
typedef unsigned u32x2 __attribute__((ext_vector_type(2)));
typedef unsigned u32x4 __attribute__((ext_vector_type(4)));
typedef unsigned u32x8 __attribute__((ext_vector_type(8)));
#define LAS __attribute__((address_space(3)))
#define DI __device__ __forceinline__

constexpr int S_ = 2048, DM = 512, NH = 32, DH = 128, HD = NH * DH, MM = 8, SEG = 32, NSEG = S_ / SEG, DF = 1376, DFP = 1408;

constexpr int NRB = 16, SKW = 8, NFS = 22;
constexpr size_t WS_PART = 0;
constexpr size_t WS_MBUF = WS_PART + 2u * 524288u;
constexpr size_t WS_CNT  = WS_MBUF + 64u * 8192u;
constexpr size_t WS_XN   = WS_CNT + 131072u;
constexpr size_t WS_Q    = WS_XN + (size_t)S_ * DM * 2;
constexpr size_t WS_K    = WS_Q + (size_t)S_ * HD * 2;
constexpr size_t WS_VT   = WS_K + (size_t)S_ * HD * 2;
constexpr size_t WS_KM   = WS_VT + (size_t)S_ * HD * 2;
constexpr size_t WS_VMT  = WS_KM + (size_t)NSEG * MM * HD * 2;
constexpr size_t WS_P    = WS_VMT + (size_t)NSEG * MM * HD * 2;
constexpr size_t WS_H    = WS_P + (size_t)SKW * S_ * DM * 4;
constexpr size_t WS_HN   = WS_H + (size_t)S_ * DM * 2;
constexpr size_t WS_ACT  = WS_HN + (size_t)S_ * DM * 2;
constexpr size_t WS_END  = WS_ACT + (size_t)S_ * DFP * 2;
constexpr int C_KV = 0, C_CH = C_KV + NRB * NH, C_Q = C_CH + 64, C_WO = C_Q + NRB * 8, C_LN = C_WO + NSEG, C_FF = C_LN + NSEG, C_NUM = C_FF + NSEG;
static_assert(C_NUM * 128 <= 131072, "counter region");
static_assert(WS_END + (1u << 20) <= (256u << 20), "workspace");

DI unsigned pk2(float a, float b) { f32x2 f = {a, b}; return __builtin_bit_cast(unsigned, __builtin_convertvector(f, bf16x2)); }
DI float bflo(unsigned u) { return __uint_as_float(u << 16); }
DI float bfhi(unsigned u) { return __uint_as_float(u & 0xffff0000u); }
DI f32x4 mfma16(bf16x8 a, bf16x8 b, f32x4 c) { return __builtin_amdgcn_mfma_f32_16x16x32_bf16(a, b, c, 0, 0, 0); }
DI f32x4 smf(bf16x8 a, bf16x16 b, f32x4 c, int idx) { return __builtin_amdgcn_smfmac_f32_16x16x64_bf16(a, b, c, idx, 0, 0); }
DI bf16x8 mk8(unsigned a, unsigned b, unsigned c, unsigned d) { u32x4 u = {a, b, c, d}; return __builtin_bit_cast(bf16x8, u); }
DI u32x2 trread(const char* lds_ptr) {
  s16x4 r = __builtin_amdgcn_ds_read_tr16_b64_v4i16((LAS s16x4*)lds_ptr);
  return __builtin_bit_cast(u32x2, r);
}
DI float xmax32(float x) { auto r = __builtin_amdgcn_permlane32_swap(__float_as_uint(x), __float_as_uint(x), false, false); return fmaxf(__uint_as_float(r[0]), __uint_as_float(r[1])); }
DI float xmax16(float x) { auto r = __builtin_amdgcn_permlane16_swap(__float_as_uint(x), __float_as_uint(x), false, false); return fmaxf(__uint_as_float(r[0]), __uint_as_float(r[1])); }
DI float xsum32(float x) { auto r = __builtin_amdgcn_permlane32_swap(__float_as_uint(x), __float_as_uint(x), false, false); return __uint_as_float(r[0]) + __uint_as_float(r[1]); }
DI float xsum16(float x) { auto r = __builtin_amdgcn_permlane16_swap(__float_as_uint(x), __float_as_uint(x), false, false); return __uint_as_float(r[0]) + __uint_as_float(r[1]); }
DI float comb(float a, float b) { auto r = __builtin_amdgcn_permlane32_swap(__float_as_uint(a), __float_as_uint(b), false, false); return __uint_as_float(r[0]) + __uint_as_float(r[1]); }

__global__ void __launch_bounds__(256) prep_kernel(const bf16* __restrict__ x, const bf16* __restrict__ mem0, char* ws) {
  const int t = threadIdx.x, lane = t & 63, w = t >> 6;
  const int row = blockIdx.x * 4 + w;
  {
    u32x4 v = *(const u32x4*)(x + (size_t)row * DM + lane * 8);
    float f[8] = {bflo(v[0]), bfhi(v[0]), bflo(v[1]), bfhi(v[1]), bflo(v[2]), bfhi(v[2]), bflo(v[3]), bfhi(v[3])};
    float s = 0;
#pragma unroll
    for (int i = 0; i < 8; ++i) s += f[i];
#pragma unroll
    for (int m = 32; m >= 1; m >>= 1) s += __shfl_xor(s, m);
    const float mean = s * (1.f / DM);
    float q = 0;
#pragma unroll
    for (int i = 0; i < 8; ++i) { f[i] -= mean; q += f[i] * f[i]; }
#pragma unroll
    for (int m = 32; m >= 1; m >>= 1) q += __shfl_xor(q, m);
    const float rs = 1.f / sqrtf(q * (1.f / DM) + 1e-5f);
    u32x4 o = {pk2(f[0] * rs, f[1] * rs), pk2(f[2] * rs, f[3] * rs), pk2(f[4] * rs, f[5] * rs), pk2(f[6] * rs, f[7] * rs)};
    *(u32x4*)(ws + WS_XN + ((size_t)row * DM + lane * 8) * 2) = o;
  }
  const int gt = blockIdx.x * 256 + t;
  if (gt < 65536) { u32x4 z = {0, 0, 0, 0}; *(u32x4*)(ws + WS_PART + (size_t)gt * 16) = z; }
  else if (gt < 65536 + 32768) {
    const int u = gt - 65536;
    u32x4 z = {~0u, ~0u, ~0u, ~0u};
    if (u < 512) {
      const int ks = u >> 6, ga = (u >> 4) & 3, hb = (u >> 3) & 1, i = u & 7;
#pragma unroll
      for (int a = 0; a < 4; ++a) z[a] = *(const unsigned*)(mem0 + i * DM + ks * 64 + ga * 16 + 4 * a + 2 * hb);
#pragma unroll
      for (int j = 0; j < 4; ++j) if (z[j] == ~0u) z[j] = 0x7fff7fffu;
    }
    *(u32x4*)(ws + WS_MBUF + (size_t)u * 16) = z;
  }
  else if (gt < 65536 + 32768 + 8192) { u32x4 z = {0, 0, 0, 0}; *(u32x4*)(ws + WS_CNT + (size_t)(gt - 65536 - 32768) * 16) = z; }
}

constexpr int CL_W = 0, CL_M = 131072, CL_QK = CL_M + 8192, CL_KX = CL_QK + 4096, CL_O = CL_KX + 16384, CL_R = CL_O + 2048, CL_END = CL_R + 2048;
#define DLY1 8
#define DLY2 12
constexpr unsigned SPIN_CAP = 1u << 18;
#define ACLOB "a0","a1","a2","a3","a4","a5","a6","a7","a8","a9","a10","a11","a12","a13","a14","a15","a16","a17","a18","a19","a20","a21","a22","a23","a24","a25","a26","a27","a28","a29","a30","a31","a32","a33","a34","a35","a36","a37","a38","a39","a40","a41","a42","a43","a44","a45","a46","a47","a48","a49","a50","a51","a52","a53","a54","a55","a56","a57","a58","a59","a60","a61","a62","a63","a64","a65","a66","a67","a68","a69","a70","a71","a72","a73","a74","a75","a76","a77","a78","a79","a80","a81","a82","a83","a84","a85","a86","a87","a88","a89","a90","a91","a92","a93","a94","a95","a96","a97","a98","a99","a100","a101","a102","a103","a104","a105","a106","a107","a108","a109","a110","a111","a112","a113","a114","a115","a116","a117","a118","a119","a120","a121","a122","a123","a124","a125","a126","a127","a128","a129","a130","a131","a132","a133","a134","a135","a136","a137","a138","a139","a140","a141","a142","a143","a144","a145","a146","a147","a148","a149","a150","a151","a152","a153","a154","a155","a156","a157","a158","a159","a160","a161","a162","a163","a164","a165","a166","a167","a168","a169","a170","a171","a172","a173","a174","a175","a176","a177","a178","a179","a180","a181","a182","a183","a184","a185","a186","a187","a188","a189","a190","a191","a192","a193","a194","a195","a196","a197","a198","a199","a200","a201","a202","a203","a204","a205","a206","a207","a208","a209","a210","a211","a212","a213","a214","a215","a216","a217","a218","a219","a220","a221","a222","a223","a224","a225","a226","a227","a228","a229","a230","a231","a232","a233","a234","a235","a236","a237","a238","a239","a240","a241","a242","a243","a244","a245","a246","a247","a248","a249","a250","a251","a252","a253","a254","a255"

DI int lane_id() { int l; asm volatile("v_mbcnt_lo_u32_b32 %0, -1, 0\n\tv_mbcnt_hi_u32_b32 %0, -1, %0" : "=v"(l)); return l; }
DI float max3f(float a, float b, float c) { float r; asm("v_max3_f32 %0, %1, %2, %3" : "=v"(r) : "v"(a), "v"(b), "v"(c)); return r; }
DI float max2f(float a, float b) { float r; asm("v_max_f32_e32 %0, %1, %2" : "=v"(r) : "v"(a), "v"(b)); return r; }
DI void st16_sc1(void* p, u32x4 v) { asm volatile("global_store_dwordx4 %0, %1, off sc1\n\ts_nop 1" ::"v"(p), "v"(v) : "memory"); }
DI void st4_sc1(void* p, unsigned v) { asm volatile("global_store_dword %0, %1, off sc1" ::"v"(p), "v"(v) : "memory"); }
DI void dma16(const void* src, unsigned dst) {
  asm volatile("s_mov_b32 m0, %1\n\ts_nop 0\n\tglobal_load_lds_dwordx4 %0, off" ::"v"(src), "s"(dst) : "memory", "m0");
}
template <int N, int OFF> DI void tr_to_acc(unsigned addr) {
  asm volatile("ds_read_b64_tr_b16 a[%c1:%c2], %0 offset:%c3" ::"v"(addr), "i"(N), "i"(N + 1), "i"(OFF) : "memory", ACLOB);
}
template <int A0, int BUF> DI void chunk_to_acc(unsigned addr) {
  tr_to_acc<A0 + 0, BUF + 0>(addr); tr_to_acc<A0 + 2, BUF + 256>(addr); tr_to_acc<A0 + 4, BUF + 2048>(addr); tr_to_acc<A0 + 6, BUF + 2304>(addr);
  tr_to_acc<A0 + 8, BUF + 32>(addr); tr_to_acc<A0 + 10, BUF + 288>(addr); tr_to_acc<A0 + 12, BUF + 2080>(addr); tr_to_acc<A0 + 14, BUF + 2336>(addr);
}
template <int KS> DI void qk_step(f32x4& q0, f32x4& q1, f32x4& k0, f32x4& k1, bf16x8 m, int sidx) {
  asm volatile("v_smfmac_f32_16x16x64_bf16 %0, %4, a[%c6:%c7], %5\n\t"
               "v_smfmac_f32_16x16x64_bf16 %1, %4, a[%c8:%c9], %5\n\t"
               "v_smfmac_f32_16x16x64_bf16 %2, %4, a[%c10:%c11], %5\n\t"
               "v_smfmac_f32_16x16x64_bf16 %3, %4, a[%c12:%c13], %5"
               : "+v"(q0), "+v"(q1), "+v"(k0), "+v"(k1)
               : "v"(m), "v"(sidx), "i"(KS * 32), "i"(KS * 32 + 7), "i"(KS * 32 + 8), "i"(KS * 32 + 15), "i"(KS * 32 + 16), "i"(KS * 32 + 23), "i"(KS * 32 + 24), "i"(KS * 32 + 31)
               : ACLOB);
}
DI void smf2(f32x4& d0, f32x4& d1, bf16x8 m, u32x8 w0, u32x8 w1, int sidx) {
  asm volatile("v_smfmac_f32_16x16x64_bf16 %0, %2, %3, %5\n\tv_smfmac_f32_16x16x64_bf16 %1, %2, %4, %5"
               : "+v"(d0), "+v"(d1) : "v"(m), "v"(w0), "v"(w1), "v"(sidx));
}
#define STAMP(i)

DI unsigned ld4_sc1(const void* p) { unsigned r; asm volatile("global_load_dword %0, %1, off sc1\n\ts_waitcnt vmcnt(0)" : "=&v"(r) : "v"(p) : "memory"); return r; }
DI void st8_sc1(void* p, u32x2 v) { asm volatile("global_store_dwordx2 %0, %1, off sc1\n\ts_nop 1" ::"v"(p), "v"(v) : "memory"); }

DI void chain_role(const bf16* __restrict__ Wq, const bf16* __restrict__ Wk, const bf16* __restrict__ Wv,
                   const bf16* __restrict__ Wo, char* ws, char* lds, const int wv) {
  asm volatile("; CHAIN_BEGIN");
  const int cu = blockIdx.x, h = cu >> 1, jh = cu & 1;
  const int lane = lane_id(), w = wv, t = w * 64 + lane, c = lane & 15, g = lane >> 4;
  u32x8 wvr[4][2];
  const unsigned ldsbase = (unsigned)(size_t)(LAS char*)lds;
  char* const wl = lds + CL_W + w * 32768 + lane * 16;
  {
    const unsigned stg = ldsbase + CL_W + w * 32768;
    const size_t soff = (size_t)(lane >> 2) * HD + h * 128 + w * 32 + (lane & 3) * 8;
    auto issue = [&](const bf16* W, int ks, int buf) {
#pragma unroll
      for (int i = 0; i < 4; ++i) dma16(W + (size_t)(ks * 64 + i * 16) * HD + soff, stg + buf * 4096 + i * 1024);
    };
    const unsigned ta = stg + (8 * g + (c >> 2)) * 64 + (c & 3) * 8;
#define WAITV(n) asm volatile("s_waitcnt vmcnt(" #n ")" ::: "memory")
#define WAITL() asm volatile("s_waitcnt lgkmcnt(0)" ::: "memory")
    issue(Wq, 0, 0); issue(Wk, 0, 1); issue(Wq, 1, 2); issue(Wk, 1, 3); issue(Wq, 2, 4); issue(Wk, 2, 5); issue(Wq, 3, 6); issue(Wk, 3, 7);
    WAITV(24); chunk_to_acc<0, 0>(ta); chunk_to_acc<16, 4096>(ta); WAITL(); issue(Wq, 4, 0); issue(Wk, 4, 1);
    WAITV(24); chunk_to_acc<32, 8192>(ta); chunk_to_acc<48, 12288>(ta); WAITL(); issue(Wq, 5, 2); issue(Wk, 5, 3);
    WAITV(24); chunk_to_acc<64, 16384>(ta); chunk_to_acc<80, 20480>(ta); WAITL(); issue(Wq, 6, 4); issue(Wk, 6, 5);
    WAITV(24); chunk_to_acc<96, 24576>(ta); chunk_to_acc<112, 28672>(ta); WAITL(); issue(Wq, 7, 6); issue(Wk, 7, 7);
    WAITV(24); chunk_to_acc<128, 0>(ta); chunk_to_acc<144, 4096>(ta); WAITL(); issue(Wv, 0, 0); issue(Wv, 1, 1);
    WAITV(24); chunk_to_acc<160, 8192>(ta); chunk_to_acc<176, 12288>(ta); WAITL(); issue(Wv, 2, 2); issue(Wv, 3, 3);
    WAITV(24); chunk_to_acc<192, 16384>(ta); chunk_to_acc<208, 20480>(ta); WAITL(); issue(Wv, 4, 4); issue(Wv, 5, 5);
    WAITV(24); chunk_to_acc<224, 24576>(ta); chunk_to_acc<240, 28672>(ta); WAITL(); issue(Wv, 6, 6); issue(Wv, 7, 7);
    const char* tv = lds + CL_W + w * 32768 + (8 * g + (c >> 2)) * 64 + (c & 3) * 16;
    auto rdv = [&](int buf, u32x8 (&d)[2]) {
#pragma unroll
      for (int e = 0; e < 2; ++e)
#pragma unroll
        for (int j = 0; j < 4; ++j) { u32x2 r = trread(tv + buf * 4096 + (j >> 1) * 2048 + (j & 1) * 256 + e * 8); d[e][2 * j] = r[0]; d[e][2 * j + 1] = r[1]; }
    };
    WAITV(16);
#pragma unroll
    for (int ks = 0; ks < 4; ++ks) rdv(ks, wvr[ks]);
    u32x8 tmp[4][2];
    WAITV(0);
#pragma unroll
    for (int ks = 0; ks < 4; ++ks) rdv(4 + ks, tmp[ks]);
    asm volatile("s_waitcnt lgkmcnt(0)" : "+v"(tmp[0][0]), "+v"(tmp[0][1]), "+v"(tmp[1][0]), "+v"(tmp[1][1]), "+v"(tmp[2][0]), "+v"(tmp[2][1]), "+v"(tmp[3][0]), "+v"(tmp[3][1]),
                 "+v"(wvr[0][0]), "+v"(wvr[0][1]), "+v"(wvr[1][0]), "+v"(wvr[1][1]), "+v"(wvr[2][0]), "+v"(wvr[2][1]), "+v"(wvr[3][0]), "+v"(wvr[3][1]) :: "memory");
    {
      const bf16* src = Wo + (size_t)(h * 128 + (lane >> 3)) * DM + jh * 256 + w * 64 + (lane & 7) * 8;
#pragma unroll
      for (int i = 0; i < 16; ++i) dma16(src + (size_t)i * 8 * DM, stg + 16384 + i * 1024);
    }
#pragma unroll
    for (int ks = 0; ks < 4; ++ks)
#pragma unroll
      for (int e = 0; e < 2; ++e) {
        char* p0 = wl + ks * 4096 + e * 2048;
        *(u32x4*)p0 = (u32x4){tmp[ks][e][0], tmp[ks][e][1], tmp[ks][e][2], tmp[ks][e][3]};
        *(u32x4*)(p0 + 1024) = (u32x4){tmp[ks][e][4], tmp[ks][e][5], tmp[ks][e][6], tmp[ks][e][7]};
      }
    WAITV(0);
    const char* to = lds + CL_W + w * 32768 + 16384 + (8 * g + (c >> 2)) * 128 + (c & 3) * 32;
#pragma unroll
    for (int T = 0; T < 4; ++T)
#pragma unroll
      for (int ks = 0; ks < 2; ++ks)
#pragma unroll
        for (int j = 0; j < 4; ++j) { u32x2 r = trread(to + ks * 8192 + (j >> 1) * 4096 + (j & 1) * 512 + T * 8); tmp[T][ks][2 * j] = r[0]; tmp[T][ks][2 * j + 1] = r[1]; }
    asm volatile("s_waitcnt lgkmcnt(0)" : "+v"(tmp[0][0]), "+v"(tmp[0][1]), "+v"(tmp[1][0]), "+v"(tmp[1][1]), "+v"(tmp[2][0]), "+v"(tmp[2][1]), "+v"(tmp[3][0]), "+v"(tmp[3][1]) :: "memory");
#pragma unroll
    for (int T = 0; T < 4; ++T)
#pragma unroll
      for (int ks = 0; ks < 2; ++ks) {
        char* p0 = wl + 16384 + (T * 2 + ks) * 2048;
        *(u32x4*)p0 = (u32x4){tmp[T][ks][0], tmp[T][ks][1], tmp[T][ks][2], tmp[T][ks][3]};
        *(u32x4*)(p0 + 1024) = (u32x4){tmp[T][ks][4], tmp[T][ks][5], tmp[T][ks][6], tmp[T][ks][7]};
      }
  }
  const int sidx = (c & 8) ? 0xEEEE : 0x4444;
  const bf16* Kx = (const bf16*)(ws + WS_K) + (size_t)c * HD + h * 128 + w * 32 + 8 * g;
  const bf16* Vt = (const bf16*)(ws + WS_VT) + ((size_t)h * DH + w * 32 + 8 * (c >> 2) + (c & 3)) * 32 + 8 * g;
  const float sc = 0.08838834764831845f * 1.4426950408889634f;
  u32x8 wr1[8];
  auto dmakv = [&](int sn) {
    const bf16* kp = Kx + (size_t)sn * 32 * HD; const bf16* vp = Vt + (size_t)sn * NH * DH * 32;
    const unsigned d0 = ldsbase + CL_KX + w * 1024;
    unsigned keep;
    asm volatile("s_mov_b32 %0, m0\n\t"
                 "s_mov_b32 m0, %4\n\ts_nop 0\n\tglobal_load_lds_dwordx4 %1, off sc1\n\t"
                 "s_add_u32 m0, m0, 4096\n\ts_nop 0\n\tglobal_load_lds_dwordx4 %2, off sc1\n\t"
                 "s_add_u32 m0, m0, 4096\n\ts_nop 0\n\tglobal_load_lds_dwordx4 %3, off sc1\n\t"
                 "s_add_u32 m0, m0, 3840\n\ts_nop 0\n\tglobal_load_lds_dwordx4 %3, off offset:256 sc1\n\t"
                 "s_mov_b32 m0, %0"
                 : "=&s"(keep) : "v"(kp), "v"(kp + (size_t)16 * HD), "v"(vp), "s"(d0) : "memory", "scc");
  };
  auto ldw = [&](int off) {
#pragma unroll
    for (int i = 0; i < 8; ++i) {
      u32x4 a = *(const u32x4*)(wl + off + i * 2048), b = *(const u32x4*)(wl + off + i * 2048 + 1024);
      wr1[i] = (u32x8){a[0], a[1], a[2], a[3], b[0], b[1], b[2], b[3]};
    }
  };
  unsigned cap = SPIN_CAP;
  const char* const kvflag = ws + WS_CNT + (size_t)(C_KV + h) * 128;
  auto waitkv = [&](int rb) {
    for (unsigned spins = 0; __builtin_amdgcn_readfirstlane(ld4_sc1(kvflag + (size_t)rb * NH * 128)) < 2u; ++spins) {
      if (spins >= cap) { cap = 0; break; }
      __builtin_amdgcn_s_sleep(2);
    }
  };
  waitkv(0);
  dmakv(0);
  ldw(0);
  const unsigned ones2 = 0x3f803f80u;
  const bf16x8 onesx = mk8(ones2, ones2, ones2, ones2);
  const bf16x8 onesm = mk8(g < 2 ? ones2 : 0u, g < 2 ? ones2 : 0u, 0u, 0u);
  const unsigned tmask = ~127u;
  char* const pdst0 = ws + WS_PART + ((size_t)(32 * jh + 8 * w + (c >> 1)) * 32 + h) * 256 + ((c & 1) * 4 + (lane >> 5) * 2 + (g & 1)) * 16;
  char* const psrc0 = ws + WS_PART + (size_t)cu * 8192 + t * 16;
  auto step = [&](const int s, auto LASTC) {
    constexpr bool LAST = decltype(LASTC)::value;
    STAMP(0);
    unsigned kvv;
    {
      const char* src = ws + WS_MBUF + (size_t)s * 8192 + t * 16;
      u32x4 a, b;
      const char* fsrc = kvflag + (size_t)(LAST ? 0 : ((s + 1) >> 2)) * NH * 128;
      for (unsigned spins = 0;; ++spins) {
        asm volatile("global_load_dwordx4 %0, %3, off sc1\n\tglobal_load_dwordx4 %1, %4, off sc1\n\tglobal_load_dword %2, %5, off sc1\n\ts_waitcnt vmcnt(0)"
                     : "=&v"(a), "=&v"(b), "=&v"(kvv) : "v"(src), "v"(src + 4096), "v"(fsrc) : "memory");
        const unsigned mxu = max(max(max(a[0], a[1]), max(a[2], a[3])), max(max(b[0], b[1]), max(b[2], b[3])));
        if (__all(mxu != ~0u)) break;
        if (spins >= cap) { cap = 0; break; }
      }
      *(u32x4*)(lds + CL_M + t * 16) = a;
      *(u32x4*)(lds + CL_M + 4096 + t * 16) = b;
    }
    STAMP(1);
    __syncthreads();
    f32x4 aq[2], ak[2], av[2];
#pragma unroll
    for (int i = 0; i < 2; ++i) { aq[i] = (f32x4){0, 0, 0, 0}; ak[i] = aq[i]; av[i] = aq[i]; }
    bf16x8 mf[8];
#pragma unroll
    for (int ks = 0; ks < 8; ++ks) mf[ks] = *(const bf16x8*)(lds + CL_M + ks * 1024 + lane * 16);
    asm volatile("s_nop 1" : "+v"(aq[0]), "+v"(aq[1]), "+v"(ak[0]), "+v"(ak[1]), "+v"(av[0]), "+v"(av[1]));
    qk_step<0>(aq[0], aq[1], ak[0], ak[1], mf[0], sidx); qk_step<1>(aq[0], aq[1], ak[0], ak[1], mf[1], sidx);
    qk_step<2>(aq[0], aq[1], ak[0], ak[1], mf[2], sidx); qk_step<3>(aq[0], aq[1], ak[0], ak[1], mf[3], sidx);
    qk_step<4>(aq[0], aq[1], ak[0], ak[1], mf[4], sidx); qk_step<5>(aq[0], aq[1], ak[0], ak[1], mf[5], sidx);
    qk_step<6>(aq[0], aq[1], ak[0], ak[1], mf[6], sidx); qk_step<7>(aq[0], aq[1], ak[0], ak[1], mf[7], sidx);
    smf2(av[0], av[1], mf[4], wr1[0], wr1[1], sidx);
    asm volatile("s_nop 7" : "+v"(aq[0]), "+v"(aq[1]), "+v"(ak[0]), "+v"(ak[1]));
    STAMP(2);
    {
      const int off = (w * 32 + 16 * (lane >> 5) + c) * 16 + (g & 1) * 8;
      u32x2 qq = {pk2(comb(aq[0][0], aq[1][0]), comb(aq[0][1], aq[1][1])), pk2(comb(aq[0][2], aq[1][2]), comb(aq[0][3], aq[1][3]))};
      smf2(av[0], av[1], mf[5], wr1[2], wr1[3], sidx);
      u32x2 kk = {pk2(comb(ak[0][0], ak[1][0]), comb(ak[0][1], ak[1][1])), pk2(comb(ak[0][2], ak[1][2]), comb(ak[0][3], ak[1][3]))};
      *(u32x2*)(lds + CL_QK + off) = qq; *(u32x2*)(lds + CL_QK + 2048 + off) = kk;
    }
    smf2(av[0], av[1], mf[6], wr1[4], wr1[5], sidx);
    smf2(av[0], av[1], mf[7], wr1[6], wr1[7], sidx);
    bf16x8 kxf[2][4], vx[2];
#pragma unroll
    for (int x = 0; x < 2; ++x) {
#pragma unroll
      for (int ks = 0; ks < 4; ++ks) kxf[x][ks] = *(const bf16x8*)(lds + CL_KX + (x * 4 + ks) * 1024 + lane * 16);
      vx[x] = *(const bf16x8*)(lds + CL_KX + 8192 + (x * 4 + w) * 1024 + lane * 16);
    }
    STAMP(3);
    __syncthreads();
    STAMP(4);
    bf16x8 qf[4], kf[4];
    {
      const int off = (8 * g + (c >> 2)) * 16 + (c & 1) * 8;
#pragma unroll
      for (int ks = 0; ks < 4; ++ks) {
        u32x2 a = trread(lds + CL_QK + ks * 512 + off), b = trread(lds + CL_QK + ks * 512 + off + 64);
        qf[ks] = mk8(a[0], a[1], b[0], b[1]);
        a = trread(lds + CL_QK + 2048 + ks * 512 + off); b = trread(lds + CL_QK + 2048 + ks * 512 + off + 64);
        kf[ks] = mk8(a[0], a[1], b[0], b[1]);
      }
    }
    smf2(av[0], av[1], mf[0], wvr[0][0], wvr[0][1], sidx);
    smf2(av[0], av[1], mf[1], wvr[1][0], wvr[1][1], sidx);
    if constexpr (!LAST) {
      if (__builtin_expect(__builtin_amdgcn_readfirstlane(kvv) < 2u, 0)) waitkv((s + 1) >> 2);
      dmakv(s + 1);
    }
    smf2(av[0], av[1], mf[2], wvr[2][0], wvr[2][1], sidx);
    smf2(av[0], av[1], mf[3], wvr[3][0], wvr[3][1], sidx);
    auto publish_kv = [&]() {
      if (jh == 0) {
        if (w == 0 && c < 8) {
          bf16* kd = (bf16*)(ws + WS_KM) + (size_t)(s * 8 + c) * HD + h * 128 + 8 * g;
#pragma unroll
          for (int ks = 0; ks < 4; ++ks) st16_sc1(kd + ks * 32, __builtin_bit_cast(u32x4, kf[ks]));
        }
      } else {
        u32x2 vv = {pk2(comb(av[0][0], av[1][0]), comb(av[0][1], av[1][1])), pk2(comb(av[0][2], av[1][2]), comb(av[0][3], av[1][3]))};
        st8_sc1((bf16*)(ws + WS_VMT) + (((size_t)s * NH + h) * DH + w * 32 + 8 * (c >> 2) + 4 * (lane >> 5) + (c & 3)) * 8 + (g & 1) * 4, vv);
      }
    };
    if constexpr (LAST) {
      asm volatile("s_nop 7" : "+v"(av[0]), "+v"(av[1]));
      publish_kv();
      asm volatile("s_waitcnt vmcnt(0)" ::: "memory");
      __syncthreads();
      if (t == 0) st4_sc1(ws + WS_CNT + (size_t)(C_CH + cu) * 128, (unsigned)(s + 1));
      return;
    }
    f32x4 sm[3];
    asm volatile("s_nop 1\n\tv_mfma_f32_16x16x32_bf16 %0, %3, %6, 0\n\tv_mfma_f32_16x16x32_bf16 %1, %4, %6, 0\n\tv_mfma_f32_16x16x32_bf16 %2, %5, %6, 0"
                 : "=&v"(sm[0]), "=&v"(sm[1]), "=&v"(sm[2]) : "v"(kf[0]), "v"(kxf[0][0]), "v"(kxf[1][0]), "v"(qf[0]));
#pragma unroll
    for (int ks = 1; ks < 4; ++ks)
      asm volatile("s_nop 1\n\tv_mfma_f32_16x16x32_bf16 %0, %3, %6, %0\n\tv_mfma_f32_16x16x32_bf16 %1, %4, %6, %1\n\tv_mfma_f32_16x16x32_bf16 %2, %5, %6, %2"
                   : "+v"(sm[0]), "+v"(sm[1]), "+v"(sm[2]) : "v"(kf[ks]), "v"(kxf[0][ks]), "v"(kxf[1][ks]), "v"(qf[ks]));
    asm volatile("" : "+v"(av[0]), "+v"(av[1]));
    const bf16x8 vm0 = mk8(pk2(av[0][0], av[0][1]), pk2(av[0][2], av[0][3]), 0u, 0u);
    const bf16x8 vm1 = mk8(pk2(av[1][0], av[1][1]), pk2(av[1][2], av[1][3]), 0u, 0u);
    ldw(16384);
    asm volatile("s_nop 7" : "+v"(sm[0]), "+v"(sm[1]), "+v"(sm[2]));
    STAMP(10);
    float mx = max3f(max3f(sm[0][0], sm[0][1], sm[0][2]), max3f(sm[0][3], sm[1][0], sm[1][1]), max3f(max3f(sm[1][2], sm[1][3], sm[2][0]), max3f(sm[2][1], sm[2][2], sm[2][3]), sm[2][3]));
    {
      auto r = __builtin_amdgcn_permlane16_swap(__float_as_uint(mx), __float_as_uint(mx), false, false);
      mx = max2f(__uint_as_float(r[0]), __uint_as_float(r[1]));
      r = __builtin_amdgcn_permlane32_swap(__float_as_uint(mx), __float_as_uint(mx), false, false);
      mx = max2f(__uint_as_float(r[0]), __uint_as_float(r[1]));
    }
    const float nm = -mx * sc;
#pragma unroll
    for (int j = 0; j < 3; ++j)
#pragma unroll
      for (int r = 0; r < 4; ++r) sm[j][r] = __builtin_amdgcn_exp2f(__builtin_fmaf(sm[j][r], sc, nm));
    const bf16x8 px = mk8(pk2(sm[1][0], sm[1][1]), pk2(sm[1][2], sm[1][3]), pk2(sm[2][0], sm[2][1]), pk2(sm[2][2], sm[2][3]));
    const bf16x8 pm = mk8(pk2(sm[0][0], sm[0][1]), pk2(sm[0][2], sm[0][3]), 0u, 0u);
    STAMP(5);
    {
      f32x4 o0, o1, rs;
      asm volatile("s_nop 1\n\tv_mfma_f32_16x16x32_bf16 %2, %9, %7, 0\n\tv_mfma_f32_16x16x32_bf16 %0, %3, %7, 0\n\tv_mfma_f32_16x16x32_bf16 %2, %10, %8, %2\n\t"
                   "v_mfma_f32_16x16x32_bf16 %1, %4, %7, 0\n\tv_mfma_f32_16x16x32_bf16 %0, %5, %8, %0\n\tv_mfma_f32_16x16x32_bf16 %1, %6, %8, %1\n\ts_nop 4"
                   : "=&v"(o0), "=&v"(o1), "=&v"(rs) : "v"(vx[0]), "v"(vx[1]), "v"(vm0), "v"(vm1), "v"(px), "v"(pm), "v"(onesx), "v"(onesm));
      float inv = __builtin_amdgcn_rcpf(rs[0]);
      asm volatile("s_nop 2" : "+v"(o0), "+v"(o1), "+v"(inv));
      const bool hb = c >= 8;
      const float e0 = hb ? o0[2] : o0[0], e1 = hb ? o0[3] : o0[1], e2 = hb ? o1[2] : o1[0], e3 = hb ? o1[3] : o1[1];
      u32x2 o = {pk2(e0 * inv, e1 * inv), pk2(e2 * inv, e3 * inv)};
      *(u32x2*)(lds + CL_O + (w >> 1) * 1024 + (16 * (2 * (w & 1) + (g >> 1)) + c) * 16 + (g & 1) * 8) = o;
    }
    __syncthreads();
    STAMP(6);
    const size_t slot = (size_t)(s & 1) * 524288;
    {
      const bf16x8 of0 = *(const bf16x8*)(lds + CL_O + lane * 16), of1 = *(const bf16x8*)(lds + CL_O + 1024 + lane * 16);
      f32x4 d[4];
#pragma unroll
      for (int T = 0; T < 4; ++T) d[T] = (f32x4){0, 0, 0, 0};
      char* dst = pdst0 + slot;
      asm volatile("s_nop 1" : "+v"(d[0]), "+v"(d[1]), "+v"(d[2]), "+v"(d[3]), "+v"(dst));
      smf2(d[0], d[1], of0, wr1[0], wr1[2], sidx); smf2(d[0], d[1], of1, wr1[1], wr1[3], sidx);
      smf2(d[2], d[3], of0, wr1[4], wr1[6], sidx); smf2(d[2], d[3], of1, wr1[5], wr1[7], sidx);
      asm volatile("s_nop 4" : "+v"(d[0]), "+v"(d[1]));
      const unsigned tag = (unsigned)(s + 1);
#pragma unroll
      for (int tp = 0; tp < 2; ++tp) {
        u32x4 v;
#pragma unroll
        for (int r = 0; r < 4; ++r) asm("v_and_or_b32 %0, %1, %2, %3" : "=v"(v[r]) : "v"(comb(d[2 * tp][r], d[2 * tp + 1][r])), "v"(tmask), "s"(tag));
        if (tp == 0) { asm volatile("global_store_dwordx4 %0, %1, off sc1\n\ts_nop 1" ::"v"(dst), "v"(v) : "memory"); asm volatile("" : "+v"(d[2]), "+v"(d[3])); }
        else asm volatile("global_store_dwordx4 %0, %1, off offset:128 sc1\n\ts_nop 1" ::"v"(dst), "v"(v) : "memory");
      }
    }
    STAMP(7);
    publish_kv();
    ldw(0);
#pragma unroll 1
    for (int k = 0; k < DLY1; ++k) __builtin_amdgcn_s_sleep(1);
    STAMP(11);
    {
      char* src = psrc0 + slot;
      u32x4 r0, r1;
      const unsigned tag = (unsigned)(s + 1);
      for (unsigned spins = 0;; ++spins) {
        asm volatile("global_load_dwordx4 %0, %2, off sc1\n\tglobal_load_dwordx4 %1, %3, off sc1\n\ts_waitcnt vmcnt(0)"
                     : "=&v"(r0), "=&v"(r1) : "v"(src), "v"(src + 4096) : "memory");
        unsigned bad = 0;
#pragma unroll
        for (int j = 0; j < 4; ++j) bad |= (r0[j] ^ tag) | (r1[j] ^ tag);
        if (__all((bad & 127u) == 0)) break;
        if (spins >= cap) { cap = 0; break; }
      }
      STAMP(8);
      float x0 = __uint_as_float(r0[0]) + __uint_as_float(r1[0]), x1 = __uint_as_float(r0[1]) + __uint_as_float(r1[1]);
      float x2 = __uint_as_float(r0[2]) + __uint_as_float(r1[2]), x3 = __uint_as_float(r0[3]) + __uint_as_float(r1[3]);
      const float y0 = comb(x0, x2), y1 = comb(x1, x3);
      auto rr = __builtin_amdgcn_permlane16_swap(__float_as_uint(y0), __float_as_uint(y1), false, false);
      const float z = __uint_as_float(rr[0]) + __uint_as_float(rr[1]);
      *(float*)(lds + CL_R + w * 256 + c * 16 + g * 4) = z;
      __syncthreads();
      if (t < 32) {
        const int i = t >> 2, cp = t & 3;
        const float* R = (const float*)(lds + CL_R) + ((cp >> 1) * 8 + (cp & 1) * 2 + (i >> 2)) * 4 + (i & 3);
        const float a = (R[0] + R[64]) + (R[128] + R[192]);
        const float b = (R[16] + R[80]) + (R[144] + R[208]);
        unsigned o = pk2(a, b);
        if (o == ~0u) o = 0x7fff7fffu;
        st4_sc1(ws + WS_MBUF + (size_t)(s + 1) * 8192 + cu * 128 + t * 4, o);
      }
      if (t == 64) st4_sc1(ws + WS_CNT + (size_t)(C_CH + cu) * 128, (unsigned)(s + 1));
      STAMP(9);
      const u32x4 zz = {0, 0, 0, 0};
      st16_sc1(src, zz); st16_sc1(src + 4096, zz);
    }
#pragma unroll 1
    for (int k = 0; k < DLY2; ++k) __builtin_amdgcn_s_sleep(1);
  };
#pragma unroll 1
  for (int s = 0; s < NSEG - 1; ++s) step(s, std::false_type{});
  step(NSEG - 1, std::true_type{});
}

DI unsigned off_b(unsigned row, unsigned ch) { return 256u * row + 16u * (ch ^ (((row & 3) << 2) | ((row >> 2) & 3))); }
DI void dma16_sc1(const void* src, unsigned dst) {
  asm volatile("s_mov_b32 m0, %1\n\ts_nop 0\n\tglobal_load_lds_dwordx4 %0, off sc1" ::"v"(src), "s"(dst) : "memory", "m0");
}
template <int N> DI void waitv() { asm volatile("s_waitcnt vmcnt(%c0)" ::"i"(N) : "memory"); }

struct Frags { bf16x8 a[2][4], b[2][4]; };
template <bool WA>
DI void proj_tile(const bf16* __restrict__ A, const bf16* __restrict__ B, char* lds, f32x4 (&acc)[4][4], const int wv) {
  const int lane = lane_id(), w = wv, t = w * 64 + lane, wr = w >> 1, wc = w & 1, c = lane & 15, g = lane >> 4;
  const unsigned ldsbase = (unsigned)(size_t)(LAS char*)lds;
  const bf16* asrc = A + (size_t)(t >> 3) * DM + (((t & 7) ^ ((t >> 3) & 7)) << 3);
  const int kr0 = t >> 4;
  const bf16* bsrc = B + (size_t)kr0 * HD + (((t & 15) ^ (((kr0 & 3) << 2) | ((kr0 >> 2) & 3))) << 3);
  auto stage = [&](int kt) {
    const unsigned d = ldsbase + (kt % 5) * 32768 + w * 1024;
#pragma unroll
    for (int i = 0; i < 4; ++i) {
      dma16(asrc + (size_t)i * 32 * DM + kt * 64, d + i * 4096);
      dma16(bsrc + (size_t)(kt * 64 + i * 16) * HD, d + 16384 + i * 4096);
    }
  };
  const int q = c >> 2, p = c & 3;
  auto rd = [&](int kt, Frags& F) {
    const char* la = lds + (kt % 5) * 32768; const char* lb = la + 16384;
#pragma unroll
    for (int ks = 0; ks < 2; ++ks) {
#pragma unroll
      for (int mi = 0; mi < 4; ++mi) {
        const int r = wr * 64 + mi * 16 + c;
        F.a[ks][mi] = *(const bf16x8*)(la + r * 128 + (((ks * 4 + g) ^ (r & 7)) << 4));
      }
#pragma unroll
      for (int ni = 0; ni < 4; ++ni) {
        const int ch = WA ? wc * 8 + (ni >> 1) * 4 + p : wc * 8 + 2 * ni + (p >> 1);
        const int hf = WA ? (ni & 1) : (p & 1);
        u32x2 lo = trread(lb + off_b(32 * ks + 8 * g + q, ch) + 8 * hf);
        u32x2 hi = trread(lb + off_b(32 * ks + 8 * g + 4 + q, ch) + 8 * hf);
        F.b[ks][ni] = mk8(lo[0], lo[1], hi[0], hi[1]);
      }
    }
  };
  auto mm = [&](const Frags& F) {
#pragma unroll
    for (int ks = 0; ks < 2; ++ks)
#pragma unroll
      for (int mi = 0; mi < 4; ++mi)
#pragma unroll
        for (int ni = 0; ni < 4; ++ni)
          acc[mi][ni] = WA ? mfma16(F.b[ks][ni], F.a[ks][mi], acc[mi][ni]) : mfma16(F.a[ks][mi], F.b[ks][ni], acc[mi][ni]);
  };
#pragma unroll
  for (int i = 0; i < 4; ++i)
#pragma unroll
    for (int j = 0; j < 4; ++j) acc[i][j] = (f32x4){0, 0, 0, 0};
  Frags F0, F1;
  stage(0); stage(1); stage(2); stage(3);
  waitv<24>(); __syncthreads(); rd(0, F0);
  waitv<16>(); __syncthreads(); stage(4); rd(1, F1); mm(F0);
  waitv<16>(); __syncthreads(); stage(5); rd(2, F0); mm(F1);
  waitv<16>(); __syncthreads(); stage(6); rd(3, F1); mm(F0);
  waitv<16>(); __syncthreads(); stage(7); rd(4, F0); mm(F1);
  waitv<16>(); __syncthreads(); rd(5, F1); mm(F0);
  waitv<8>(); __syncthreads(); rd(6, F0); mm(F1);
  waitv<0>(); __syncthreads(); rd(7, F1); mm(F0);
  mm(F1);
}

#define TRACE(id, i)
constexpr unsigned BSPIN = 1u << 18;
constexpr unsigned STEPGAP = 230;
#define PSLEEP 12
DI void wait_multi(const char* ws, int idx, unsigned target, unsigned long long& tlast, unsigned gap, const int wv) {
  if (wv == 0) {
    const char* p = ws + WS_CNT + (size_t)idx * 128;
    if (!__all(ld4_sc1(p) >= target)) {
      const unsigned long long nb = tlast + gap;
      for (unsigned spins = 0; __builtin_amdgcn_s_memrealtime() < nb && spins < 4096u; ++spins) __builtin_amdgcn_s_sleep(4);
      for (unsigned spins = 0; !__all(ld4_sc1(p) >= target) && spins < BSPIN; ++spins) __builtin_amdgcn_s_sleep(PSLEEP);
    }
    tlast = __builtin_amdgcn_s_memrealtime();
  }
  __syncthreads();
}
DI void publish_cnt(char* ws, int idx, const int wv) {
  asm volatile("s_waitcnt vmcnt(0)" ::: "memory");
  __syncthreads();
  if (wv == 0 && lane_id() == 0) __hip_atomic_fetch_add((unsigned*)(ws + WS_CNT + (size_t)idx * 128), 1u, __ATOMIC_RELAXED, __HIP_MEMORY_SCOPE_AGENT);
}
DI u32x4 pk8(f32x4 a, f32x4 b) { return (u32x4){pk2(a[0], a[1]), pk2(a[2], a[3]), pk2(b[0], b[1]), pk2(b[2], b[3])}; }
DI u32x4 bld16(__amdgpu_buffer_rsrc_t r, size_t off) { return __builtin_bit_cast(u32x4, __builtin_amdgcn_raw_buffer_load_b128(r, (unsigned)off, 0, 16)); }
DI u32x2 bld8(__amdgpu_buffer_rsrc_t r, size_t off) { return __builtin_bit_cast(u32x2, __builtin_amdgcn_raw_buffer_load_b64(r, (unsigned)off, 0, 16)); }
DI void bst16(__amdgpu_buffer_rsrc_t r, size_t off, u32x4 v) { __builtin_amdgcn_raw_buffer_store_b128(v, r, (unsigned)off, 0, 16); }
DI bf16x8 asb(u32x4 v) { return __builtin_bit_cast(bf16x8, v); }
DI float gelu_t(float x) {
  const float y = 0.7978845608028654f * (x + 0.044715f * x * x * x);
  return x * __builtin_amdgcn_rcpf(1.f + __builtin_amdgcn_exp2f(-2.f * 1.4426950408889634f * y));
}
DI float dppadd(float v, const int ctrl) {
  int r;
  switch (ctrl) {
    case 0: r = __builtin_amdgcn_update_dpp(0, __float_as_int(v), 0xB1, 0xf, 0xf, true); break;
    case 1: r = __builtin_amdgcn_update_dpp(0, __float_as_int(v), 0x4E, 0xf, 0xf, true); break;
    case 2: r = __builtin_amdgcn_update_dpp(0, __float_as_int(v), 0x141, 0xf, 0xf, true); break;
    default: r = __builtin_amdgcn_update_dpp(0, __float_as_int(v), 0x140, 0xf, 0xf, true); break;
  }
  return v + __int_as_float(r);
}
DI float wave_sum(float v) { v = dppadd(v, 0); v = dppadd(v, 1); v = dppadd(v, 2); v = dppadd(v, 3); return xsum32(xsum16(v)); }

template <int NT, int KS, class RowPtr>
DI void load_frags(bf16x8 (&wf)[NT][KS], RowPtr rowptr, char* stgp, const int colbyte[NT]) {
  const int lane = lane_id(), c = lane & 15, g = lane >> 4;
  constexpr int RB_ = NT * 32, RPP = 1024 / RB_, NP = KS * 32 / RPP;
  const unsigned stg = (unsigned)(size_t)(LAS char*)stgp;
#pragma unroll
  for (int i = 0; i < NP; ++i) dma16(rowptr(i * RPP + lane / (2 * NT), lane % (2 * NT)), stg + i * 1024);
  waitv<0>();
  const char* ta = stgp + (8 * g + (c >> 2)) * RB_;
#pragma unroll
  for (int n = 0; n < NT; ++n)
#pragma unroll
    for (int ks = 0; ks < KS; ++ks) {
      u32x2 lo = trread(ta + ks * 32 * RB_ + colbyte[n]), hi = trread(ta + (ks * 32 + 4) * RB_ + colbyte[n]);
      wf[n][ks] = mk8(lo[0], lo[1], hi[0], hi[1]);
    }
  asm volatile("s_waitcnt lgkmcnt(0)" ::: "memory");
}

template <int NT, int KS, int RS>
DI void seg_gemm(const bf16x8 (&wf)[NT][KS], const char* img, f32x4 (&acc)[2][NT]) {
  const int lane = lane_id(), c = lane & 15, g = lane >> 4;
  constexpr int BT = 4, NB = KS / BT;
  static_assert(KS % BT == 0, "k-steps");
  const char* base = img + c * RS;
  bf16x8 af[2][BT][2];
  auto rd = [&](int b, bf16x8 (&d)[BT][2]) {
#pragma unroll
    for (int k = 0; k < BT; ++k)
#pragma unroll
      for (int mi = 0; mi < 2; ++mi) d[k][mi] = *(const bf16x8*)(base + mi * 16 * RS + b * 256 + (((k * 4 + g) ^ c) << 4));
  };
#pragma unroll
  for (int i = 0; i < 2; ++i)
#pragma unroll
    for (int n = 0; n < NT; ++n) acc[i][n] = (f32x4){0, 0, 0, 0};
  rd(0, af[0]);
#pragma unroll
  for (int b = 0; b < NB; ++b) {
    if (b + 1 < NB) rd(b + 1, af[(b + 1) & 1]);
    __builtin_amdgcn_sched_barrier(0);
#pragma unroll
    for (int k = 0; k < BT; ++k)
#pragma unroll
      for (int mi = 0; mi < 2; ++mi)
#pragma unroll
        for (int n = 0; n < NT; ++n) acc[mi][n] = mfma16(wf[n][b * BT + k], af[b & 1][k][mi], acc[mi][n]);
    __builtin_amdgcn_sched_barrier(0);
  }
}

constexpr int NCHAIN = 64, NBULK = 192;
constexpr int N_AW = 64, N_FF = 2 * NFS, N_LN = 8, N_FIN = 16, N_SYS = N_AW + N_FF + N_LN + N_FIN, N_PJ = NBULK - N_SYS;
constexpr int T_PROJ = 48 * 32, PRE_ROUNDS = 3;

DI void proj_task(int ti, const bf16* __restrict__ Wq, const bf16* __restrict__ Wk, const bf16* __restrict__ Wv, char* ws, char* lds, __amdgpu_buffer_rsrc_t R, const int wv) {
  const int lane = lane_id(), w = wv, t = w * 64 + lane, wr = w >> 1, wc = w & 1, c = lane & 15, g = lane >> 4;
  f32x4 acc[4][4];
  TRACE(ti, 1);
  const int grp = ti >> 5, h = ti & 31;
  int mat, rb;
  if (grp < 4) { rb = grp >> 1; mat = 1 + (grp & 1); }
  else if (grp < 46) { const int g2 = grp - 4, r3 = g2 % 3; rb = 2 + g2 / 3; mat = r3; if (r3 == 0) rb -= 2; }
  else { mat = 0; rb = 14 + (grp - 46); }
  const bf16* A = (const bf16*)(ws + WS_XN) + (size_t)rb * 128 * DM;
  if (mat < 2) {
    proj_tile<true>(A, (mat == 0 ? Wq : Wk) + h * 128, lds, acc, wv);
    const size_t ob = mat == 0 ? WS_Q : WS_K;
#pragma unroll
    for (int mi = 0; mi < 4; ++mi)
#pragma unroll
      for (int j = 0; j < 2; ++j) {
        const int tok = rb * 128 + wr * 64 + mi * 16 + c, d = wc * 64 + j * 32 + 8 * g;
        bst16(R, ob + ((size_t)tok * HD + h * 128 + d) * 2, pk8(acc[mi][2 * j], acc[mi][2 * j + 1]));
      }
    publish_cnt(ws, mat == 0 ? C_Q + rb * 8 + (h >> 2) : C_KV + rb * NH + h, wv);
  } else {
    proj_tile<false>(A, Wv + h * 128, lds, acc, wv);
#pragma unroll
    for (int m2 = 0; m2 < 2; ++m2)
#pragma unroll
      for (int ni = 0; ni < 4; ++ni) {
        const int seg = rb * 4 + wr * 2 + m2, d = wc * 64 + ni * 16 + c;
        bst16(R, WS_VT + ((((size_t)seg * NH + h) * DH + d) * 32 + g * 8) * 2, pk8(acc[2 * m2][ni], acc[2 * m2 + 1][ni]));
      }
    publish_cnt(ws, C_KV + rb * NH + h, wv);
  }
  TRACE(ti, 2);
}

DI void aw_role(int id, const bf16* __restrict__ Wo, char* ws, char* lds, __amdgpu_buffer_rsrc_t R, const int wv) {
  const int lane = lane_id(), w = wv, t = w * 64 + lane, c = lane & 15, g = lane >> 4;
  const int par = id >> 5, hg = id & 7, nt = (id >> 3) & 3, h = hg * 4 + w;
  bf16x8 wf[2][16];
  {
    const int cb[2] = {(c & 3) * 8, 32 + (c & 3) * 8};
    const bf16* base = Wo + (size_t)hg * 512 * DM + nt * 128 + w * 32;
    load_frags<2, 16>(wf, [&](int r, int pc) { return base + (size_t)r * DM + pc * 8; }, lds + w * 32768, cb);
  }
  __syncthreads();
  const float sc = 0.08838834764831845f * 1.4426950408889634f;
  unsigned long long tl0 = 0, tl1 = 0;
#pragma unroll 1
  for (int s = par; s < NSEG; s += 2) {
    const int rb = s >> 2;
    TRACE(T_PROJ + s * 128 + (id & 31), 0);
    if ((s & 3) < 2) wait_multi(ws, lane < 4 ? C_KV + rb * NH + hg * 4 + lane : C_Q + rb * 8 + hg, lane < 4 ? 2u : 4u, tl0, 0u, wv);
    f32x4 st[3][2];
#pragma unroll
    for (int j = 0; j < 3; ++j) { st[j][0] = (f32x4){0, 0, 0, 0}; st[j][1] = st[j][0]; }
    bf16x8 qf[2][4]; u32x4 vxr[8];
    {
      bf16x8 kf[2][4];
#pragma unroll
      for (int ks = 0; ks < 4; ++ks) {
        const size_t col = h * 128 + ks * 32 + 8 * g;
#pragma unroll
        for (int x = 0; x < 2; ++x) {
          qf[x][ks] = asb(bld16(R, WS_Q + ((size_t)(s * 32 + 16 * x + c) * HD + col) * 2));
          kf[x][ks] = asb(bld16(R, WS_K + ((size_t)(s * 32 + 16 * x + c) * HD + col) * 2));
        }
      }
#pragma unroll
      for (int dt = 0; dt < 8; ++dt) {
        const size_t drow = ((size_t)s * NH + h) * DH + (dt >> 1) * 32 + 8 * (c >> 2) + 4 * (dt & 1) + (c & 3);
        vxr[dt] = bld16(R, WS_VT + (drow * 32 + 8 * g) * 2);
      }
#pragma unroll
      for (int ks = 0; ks < 4; ++ks) {
        st[1][0] = mfma16(kf[0][ks], qf[0][ks], st[1][0]); st[1][1] = mfma16(kf[0][ks], qf[1][ks], st[1][1]);
        st[2][1] = mfma16(kf[1][ks], qf[1][ks], st[2][1]);
      }
    }
    wait_multi(ws, C_CH + 8 * hg + (lane & 7), (unsigned)(s + 1), tl1, 2 * STEPGAP, wv);
    TRACE(T_PROJ + s * 128 + (id & 31), 1);
    u32x2 vmr[8];
    {
      bf16x8 kmf[4];
#pragma unroll
      for (int ks = 0; ks < 4; ++ks) kmf[ks] = asb(bld16(R, WS_KM + ((size_t)(s * 8 + (c & 7)) * HD + h * 128 + ks * 32 + 8 * g) * 2));
#pragma unroll
      for (int dt = 0; dt < 8; ++dt) {
        const size_t drow = ((size_t)s * NH + h) * DH + (dt >> 1) * 32 + 8 * (c >> 2) + 4 * (dt & 1) + (c & 3);
        vmr[dt] = bld8(R, WS_VMT + (drow * 8 + (g & 1) * 4) * 2);
      }
#pragma unroll
      for (int ks = 0; ks < 4; ++ks) { st[0][0] = mfma16(kmf[ks], qf[0][ks], st[0][0]); st[0][1] = mfma16(kmf[ks], qf[1][ks], st[0][1]); }
    }
    bf16x8 px[2], pm[2];
#pragma unroll
    for (int qt = 0; qt < 2; ++qt) {
      float v[12];
#pragma unroll
      for (int r = 0; r < 4; ++r) {
        v[r] = (g < 2) ? st[0][qt][r] : -INFINITY;
        const int kj = 4 * g + r;
        v[4 + r] = (qt == 1 || kj <= c) ? st[1][qt][r] : -INFINITY;
        v[8 + r] = (qt == 1 && kj <= c) ? st[2][qt][r] : -INFINITY;
      }
      float mx = v[0];
#pragma unroll
      for (int i = 1; i < 12; ++i) mx = fmaxf(mx, v[i]);
      mx = xmax16(mx); mx = xmax32(mx);
      float sum = 0;
#pragma unroll
      for (int i = 0; i < 12; ++i) { v[i] = __builtin_amdgcn_exp2f((v[i] - mx) * sc); sum += v[i]; }
      sum = xsum16(sum); sum = xsum32(sum);
      const float inv = __builtin_amdgcn_rcpf(sum);
      px[qt] = mk8(pk2(v[4] * inv, v[5] * inv), pk2(v[6] * inv, v[7] * inv), pk2(v[8] * inv, v[9] * inv), pk2(v[10] * inv, v[11] * inv));
      pm[qt] = mk8(pk2(v[0] * inv, v[1] * inv), pk2(v[2] * inv, v[3] * inv), 0u, 0u);
    }
#pragma unroll
    for (int dj = 0; dj < 4; ++dj)
#pragma unroll
      for (int qt = 0; qt < 2; ++qt) {
        f32x4 o[2];
#pragma unroll
        for (int e = 0; e < 2; ++e) {
          o[e] = mfma16(asb(vxr[2 * dj + e]), px[qt], (f32x4){0, 0, 0, 0});
          o[e] = mfma16(mk8(vmr[2 * dj + e][0], vmr[2 * dj + e][1], 0u, 0u), pm[qt], o[e]);
        }
        *(u32x4*)(lds + (qt * 16 + c) * 1024 + (((w * 16 + dj * 4 + g) ^ c) << 4)) = pk8(o[0], o[1]);
      }
    __syncthreads();
    f32x4 acc[2][2];
    seg_gemm<2, 16, 1024>(wf, lds, acc);
#pragma unroll
    for (int mi = 0; mi < 2; ++mi)
#pragma unroll
      for (int n = 0; n < 2; ++n)
        bst16(R, WS_P + ((((size_t)s * SKW + hg) * 32 + mi * 16 + c) * DM + nt * 128 + w * 32 + n * 16 + 4 * g) * 4, __builtin_bit_cast(u32x4, acc[mi][n]));
    publish_cnt(ws, C_WO + s, wv);
    TRACE(T_PROJ + s * 128 + (id & 31), 2);
  }
}

DI void ln_role(int id, const bf16* __restrict__ x, char* ws, __amdgpu_buffer_rsrc_t R, const int wv) {
  const int lane = lane_id(), w = wv;
  unsigned long long tl = 0;
#pragma unroll 1
  for (int s = 0; s < NSEG; ++s) {
    const int r32 = id * 4 + w, row = s * 32 + r32;
    const u32x4 v = *(const u32x4*)(x + (size_t)row * DM + lane * 8);
    wait_multi(ws, C_WO + s, 32u, tl, STEPGAP, wv);
    TRACE(T_PROJ + s * 128 + 32 + id, 1);
    f32x4 p[SKW][2];
#pragma unroll
    for (int sk = 0; sk < SKW; ++sk) {
      const size_t o = WS_P + ((((size_t)s * SKW + sk) * 32 + r32) * DM + lane * 8) * 4;
      p[sk][0] = __builtin_bit_cast(f32x4, bld16(R, o)); p[sk][1] = __builtin_bit_cast(f32x4, bld16(R, o + 16));
    }
    f32x4 a0 = ((p[0][0] + p[1][0]) + (p[2][0] + p[3][0])) + ((p[4][0] + p[5][0]) + (p[6][0] + p[7][0]));
    f32x4 a1 = ((p[0][1] + p[1][1]) + (p[2][1] + p[3][1])) + ((p[4][1] + p[5][1]) + (p[6][1] + p[7][1]));
    const u32x4 ar = pk8(a0, a1);
    u32x4 hb;
#pragma unroll
    for (int i = 0; i < 4; ++i) hb[i] = pk2(bflo(v[i]) + bflo(ar[i]), bfhi(v[i]) + bfhi(ar[i]));
    float f[8] = {bflo(hb[0]), bfhi(hb[0]), bflo(hb[1]), bfhi(hb[1]), bflo(hb[2]), bfhi(hb[2]), bflo(hb[3]), bfhi(hb[3])};
    const float mean = wave_sum(((f[0] + f[1]) + (f[2] + f[3])) + ((f[4] + f[5]) + (f[6] + f[7]))) * (1.f / DM);
    float qq = 0;
#pragma unroll
    for (int i = 0; i < 8; ++i) { f[i] -= mean; qq += f[i] * f[i]; }
    const float rs = 1.f / sqrtf(wave_sum(qq) * (1.f / DM) + 1e-5f);
    u32x4 o = {pk2(f[0] * rs, f[1] * rs), pk2(f[2] * rs, f[3] * rs), pk2(f[4] * rs, f[5] * rs), pk2(f[6] * rs, f[7] * rs)};
    bst16(R, WS_HN + ((size_t)row * DM + lane * 8) * 2, o);
    bst16(R, WS_H + ((size_t)row * DM + lane * 8) * 2, hb);
    publish_cnt(ws, C_LN + s, wv);
    TRACE(T_PROJ + s * 128 + 32 + id, 2);
  }
}

DI void f1_role(int id, const bf16* __restrict__ Wg, const bf16* __restrict__ Wu, char* ws, char* lds, __amdgpu_buffer_rsrc_t R, const int wv) {
  const int lane = lane_id(), w = wv, t = w * 64 + lane, c = lane & 15, g = lane >> 4;
  const int par = id / NFS, fs = id % NFS;
  const int f0 = fs * 64 + w * 16;
  const bool valid = f0 < DF;
  bf16x8 wgu[2][16];
  {
    const int cb[2] = {(c & 3) * 8, 32 + (c & 3) * 8};
    const int fc = valid ? f0 : 0;
    load_frags<2, 16>(wgu, [&](int r, int pc) { return (pc < 2 ? Wg : Wu) + (size_t)r * DF + fc + (pc & 1) * 8; }, lds + w * 32768, cb);
  }
  __syncthreads();
  const unsigned ldsbase = (unsigned)(size_t)(LAS char*)lds;
  unsigned long long tl = 0;
#pragma unroll 1
  for (int s = par; s < NSEG; s += 2) {
    wait_multi(ws, C_LN + s, N_LN, tl, 2 * STEPGAP, wv);
    TRACE(T_PROJ + s * 128 + 40 + fs, 1);
#pragma unroll
    for (int i = 0; i < 8; ++i) {
      const int r = w * 8 + i;
      dma16_sc1(ws + WS_HN + ((size_t)(s * 32 + r) * DM) * 2 + ((lane ^ (r & 15)) << 4), ldsbase + r * 1024);
    }
    waitv<0>();
    __syncthreads();
    f32x4 acc[2][2];
    seg_gemm<2, 16, 1024>(wgu, lds, acc);
#pragma unroll
    for (int mi = 0; mi < 2; ++mi) {
      float r[4];
#pragma unroll
      for (int j = 0; j < 4; ++j) r[j] = valid ? gelu_t(acc[mi][0][j]) * acc[mi][1][j] : 0.f;
      __builtin_amdgcn_raw_buffer_store_b64((u32x2){pk2(r[0], r[1]), pk2(r[2], r[3])}, R, (unsigned)(WS_ACT + ((size_t)(s * 32 + mi * 16 + c) * DFP + f0 + 4 * g) * 2), 0, 16);
    }
    publish_cnt(ws, C_FF + s, wv);
    TRACE(T_PROJ + s * 128 + 40 + fs, 2);
  }
}

DI void f2_role(int id, const bf16* __restrict__ Wd, char* ws, bf16* __restrict__ out, char* lds, __amdgpu_buffer_rsrc_t R, const int wv) {
  const int lane = lane_id(), w = wv, t = w * 64 + lane, c = lane & 15, g = lane >> 4;
  const int par = id >> 3, nb = id & 7, n0 = nb * 64 + w * 16;
  bf16x8 wd[1][44];
  {
    const int cb[1] = {(c & 3) * 8};
    bf16x8 (&lo)[1][22] = *reinterpret_cast<bf16x8 (*)[1][22]>(&wd[0][0]);
    bf16x8 (&hi)[1][22] = *reinterpret_cast<bf16x8 (*)[1][22]>(&wd[0][22]);
    load_frags<1, 22>(lo, [&](int r, int pc) { return Wd + (size_t)r * DM + n0 + pc * 8; }, lds + w * 32768, cb);
    load_frags<1, 22>(hi, [&](int r, int pc) { int fr = 704 + r; fr = fr < DF ? fr : DF - 1; return Wd + (size_t)fr * DM + n0 + pc * 8; }, lds + w * 32768, cb);
  }
  __syncthreads();
  const unsigned ldsbase = (unsigned)(size_t)(LAS char*)lds;
  unsigned soff[22];
#pragma unroll
  for (int i = 0; i < 22; ++i) {
    const int u = (w * 22 + i) * 64 + lane, r = u / 176, sl = u % 176;
    soff[i] = r * (DFP * 2) + (((sl & ~15) | ((sl ^ r) & 15)) << 4);
  }
  unsigned long long tl = 0;
#pragma unroll 1
  for (int s = par; s < NSEG; s += 2) {
    wait_multi(ws, C_FF + s, NFS, tl, 2 * STEPGAP, wv);
    TRACE(T_PROJ + s * 128 + 64 + nb, 1);
    const char* src = ws + WS_ACT + (size_t)s * 32 * DFP * 2;
#pragma unroll
    for (int i = 0; i < 22; ++i) dma16_sc1(src + soff[i], ldsbase + (w * 22 + i) * 1024);
    u32x2 hv[2];
#pragma unroll
    for (int mi = 0; mi < 2; ++mi) hv[mi] = bld8(R, WS_H + ((size_t)(s * 32 + mi * 16 + c) * DM + n0 + 4 * g) * 2);
    waitv<0>();
    __syncthreads();
    f32x4 acc[2][1];
    seg_gemm<1, 44, DFP * 2>(wd, lds, acc);
#pragma unroll
    for (int mi = 0; mi < 2; ++mi) {
      const unsigned d0 = pk2(acc[mi][0][0], acc[mi][0][1]), d1 = pk2(acc[mi][0][2], acc[mi][0][3]);
      u32x2 o = {pk2(bflo(hv[mi][0]) + bflo(d0), bfhi(hv[mi][0]) + bfhi(d0)), pk2(bflo(hv[mi][1]) + bflo(d1), bfhi(hv[mi][1]) + bfhi(d1))};
      *(u32x2*)(out + (size_t)(s * 32 + mi * 16 + c) * DM + n0 + 4 * g) = o;
    }
    TRACE(T_PROJ + s * 128 + 64 + nb, 2);
  }
}

__global__ void __launch_bounds__(256, 1) mega_kernel(const bf16* __restrict__ x, const bf16* __restrict__ Wq, const bf16* __restrict__ Wk, const bf16* __restrict__ Wv,
                                                      const bf16* __restrict__ Wo, const bf16* __restrict__ Wg, const bf16* __restrict__ Wu, const bf16* __restrict__ Wd,
                                                      char* ws, bf16* __restrict__ out) {
  extern __shared__ __attribute__((aligned(16))) char lds[];
  const int wv = __builtin_amdgcn_readfirstlane(threadIdx.x >> 6);
  if (blockIdx.x < NCHAIN) { chain_role(Wq, Wk, Wv, Wo, ws, lds, wv); return; }
  const int b = blockIdx.x - NCHAIN;
  const __amdgpu_buffer_rsrc_t R = __builtin_amdgcn_make_buffer_rsrc(ws, 0, (int)WS_END, 0x00020000);
  {
    int ti = b, n = 0;
#pragma unroll 1
    while (ti < T_PROJ) {
      proj_task(ti, Wq, Wk, Wv, ws, lds, R, wv);
      ++n;
      if (n < PRE_ROUNDS) ti += NBULK;
      else if (b < N_SYS) break;
      else ti = PRE_ROUNDS * NBULK + (n - PRE_ROUNDS) * N_PJ + (b - N_SYS);
    }
  }
#define EXPM 15
  if (b < N_AW) { if (EXPM & 1) aw_role(b, Wo, ws, lds, R, wv); }
  else if (b < N_AW + N_FF) { if (EXPM & 4) f1_role(b - N_AW, Wg, Wu, ws, lds, R, wv); }
  else if (b < N_AW + N_FF + N_LN) { if (EXPM & 2) ln_role(b - N_AW - N_FF, x, ws, R, wv); }
  else if (b < N_SYS) { if (EXPM & 8) f2_role(b - N_AW - N_FF - N_LN, Wd, ws, out, lds, R, wv); }
}

extern "C" void kernel_launch(void* const* d_in, const int* in_sizes, int n_in, void* d_out, int out_size, void* d_ws, size_t ws_size,
                              hipStream_t stream) {
  const bf16* x = (const bf16*)d_in[0];
  const bf16* mem0 = (const bf16*)d_in[1];
  const bf16* Wq = (const bf16*)d_in[2];
  const bf16* Wk = (const bf16*)d_in[3];
  const bf16* Wv = (const bf16*)d_in[4];
  const bf16* Wo = (const bf16*)d_in[5];
  const bf16* Wg = (const bf16*)d_in[8];
  const bf16* Wu = (const bf16*)d_in[9];
  const bf16* Wd = (const bf16*)d_in[10];
  char* ws = (char*)d_ws;
  bf16* out = (bf16*)d_out;
  (void)hipFuncSetAttribute((const void*)mega_kernel, hipFuncAttributeMaxDynamicSharedMemorySize, CL_END);
  prep_kernel<<<512, 256, 0, stream>>>(x, mem0, ws);
  mega_kernel<<<NCHAIN + NBULK, 256, CL_END, stream>>>(x, Wq, Wk, Wv, Wo, Wg, Wu, Wd, ws, out);
}
```

```cpp
#include <hip/hip_runtime.h>
#include <hip/hip_bf16.h>
#include <stdint.h>
#include <type_traits>

typedef __bf16 bf16;
typedef __bf16 bf16x2 __attribute__((ext_vector_type(2)));
typedef __bf16 bf16x4 __attribute__((ext_vector_type(4)));
typedef __bf16 bf16x8 __attribute__((ext_vector_type(8)));
typedef __bf16 bf16x16 __attribute__((ext_vector_type(16)));
typedef short s16x4 __attribute__((ext_vector_type(4)));
typedef float f32x2 __attribute__((ext_vector_type(2)));
typedef float f32x4 __attribute__((ext_vector_type(4)));
typedef unsigned u32x2 __attribute__((ext_vector_type(2)));
typedef unsigned u32x4 __attribute__((ext_vector_type(4)));
typedef unsigned u32x8 __attribute__((ext_vector_type(8)));
#define LAS __attribute__((address_space(3)))
#define DI __device__ __forceinline__

constexpr int S_ = 2048, DM = 512, NH = 32, DH = 128, HD = NH * DH, MM = 8, SEG = 32, NSEG = S_ / SEG, DF = 1376, DFP = 1408;

constexpr int NRB = 16, SKW = 8, NFS = 22;
constexpr size_t WS_PART = 0;
constexpr size_t WS_MBUF = WS_PART + 2u * 524288u;
constexpr size_t WS_CNT  = WS_MBUF + 64u * 8192u;
constexpr size_t WS_XN   = WS_CNT + 131072u;
constexpr size_t WS_Q    = WS_XN + (size_t)S_ * DM * 2;
constexpr size_t WS_K    = WS_Q + (size_t)S_ * HD * 2;
constexpr size_t WS_VT   = WS_K + (size_t)S_ * HD * 2;
constexpr size_t WS_KM   = WS_VT + (size_t)S_ * HD * 2;
constexpr size_t WS_VMT  = WS_KM + (size_t)NSEG * MM * HD * 2;
constexpr size_t WS_P    = WS_VMT + (size_t)NSEG * MM * HD * 2;
constexpr size_t WS_H    = WS_P + (size_t)SKW * S_ * DM * 4;
constexpr size_t WS_HN   = WS_H + (size_t)S_ * DM * 2;
constexpr size_t WS_ACT  = WS_HN + (size_t)S_ * DM * 2;
constexpr size_t WS_END  = WS_ACT + (size_t)S_ * DFP * 2;
constexpr int C_KV = 0, C_CH = C_KV + NRB * NH, C_Q = C_CH + 64, C_WO = C_Q + NRB * 8, C_LN = C_WO + NSEG, C_FF = C_LN + NSEG, C_E0 = C_FF + NSEG, C_NUM = C_E0 + NH;
static_assert(C_NUM * 128 <= 131072, "counter region");
static_assert(WS_END + (1u << 20) <= (256u << 20), "workspace");

DI unsigned pk2(float a, float b) { f32x2 f = {a, b}; return __builtin_bit_cast(unsigned, __builtin_convertvector(f, bf16x2)); }
DI float bflo(unsigned u) { return __uint_as_float(u << 16); }
DI float bfhi(unsigned u) { return __uint_as_float(u & 0xffff0000u); }
DI f32x4 mfma16(bf16x8 a, bf16x8 b, f32x4 c) { return __builtin_amdgcn_mfma_f32_16x16x32_bf16(a, b, c, 0, 0, 0); }
DI f32x4 smf(bf16x8 a, bf16x16 b, f32x4 c, int idx) { return __builtin_amdgcn_smfmac_f32_16x16x64_bf16(a, b, c, idx, 0, 0); }
DI bf16x8 mk8(unsigned a, unsigned b, unsigned c, unsigned d) { u32x4 u = {a, b, c, d}; return __builtin_bit_cast(bf16x8, u); }
DI u32x2 trread(const char* lds_ptr) {
  s16x4 r = __builtin_amdgcn_ds_read_tr16_b64_v4i16((LAS s16x4*)lds_ptr);
  return __builtin_bit_cast(u32x2, r);
}
DI float xmax32(float x) { auto r = __builtin_amdgcn_permlane32_swap(__float_as_uint(x), __float_as_uint(x), false, false); return fmaxf(__uint_as_float(r[0]), __uint_as_float(r[1])); }
DI float xmax16(float x) { auto r = __builtin_amdgcn_permlane16_swap(__float_as_uint(x), __float_as_uint(x), false, false); return fmaxf(__uint_as_float(r[0]), __uint_as_float(r[1])); }
DI float xsum32(float x) { auto r = __builtin_amdgcn_permlane32_swap(__float_as_uint(x), __float_as_uint(x), false, false); return __uint_as_float(r[0]) + __uint_as_float(r[1]); }
DI float xsum16(float x) { auto r = __builtin_amdgcn_permlane16_swap(__float_as_uint(x), __float_as_uint(x), false, false); return __uint_as_float(r[0]) + __uint_as_float(r[1]); }
DI float comb(float a, float b) { auto r = __builtin_amdgcn_permlane32_swap(__float_as_uint(a), __float_as_uint(b), false, false); return __uint_as_float(r[0]) + __uint_as_float(r[1]); }

__global__ void __launch_bounds__(256) prep_kernel(const bf16* __restrict__ x, const bf16* __restrict__ mem0, char* ws,
                                                   const bf16* __restrict__ Wq, const bf16* __restrict__ Wk, const bf16* __restrict__ Wv, const bf16* __restrict__ Wo) {
  const int t = threadIdx.x, lane = t & 63, w = t >> 6;
  u32x4 wa[8];
  {
    const size_t o = ((size_t)blockIdx.x * 512 + t) * 8;
    wa[0] = *(const u32x4*)(Wk + o); wa[1] = *(const u32x4*)(Wk + o + 2048); wa[2] = *(const u32x4*)(Wv + o); wa[3] = *(const u32x4*)(Wv + o + 2048);
    wa[4] = *(const u32x4*)(Wq + o); wa[5] = *(const u32x4*)(Wq + o + 2048); wa[6] = *(const u32x4*)(Wo + o); wa[7] = *(const u32x4*)(Wo + o + 2048);
  }
  const int row = blockIdx.x * 4 + w;
  {
    u32x4 v = *(const u32x4*)(x + (size_t)row * DM + lane * 8);
    float f[8] = {bflo(v[0]), bfhi(v[0]), bflo(v[1]), bfhi(v[1]), bflo(v[2]), bfhi(v[2]), bflo(v[3]), bfhi(v[3])};
    float s = 0;
#pragma unroll
    for (int i = 0; i < 8; ++i) s += f[i];
#pragma unroll
    for (int m = 32; m >= 1; m >>= 1) s += __shfl_xor(s, m);
    const float mean = s * (1.f / DM);
    float q = 0;
#pragma unroll
    for (int i = 0; i < 8; ++i) { f[i] -= mean; q += f[i] * f[i]; }
#pragma unroll
    for (int m = 32; m >= 1; m >>= 1) q += __shfl_xor(q, m);
    const float rs = 1.f / sqrtf(q * (1.f / DM) + 1e-5f);
    u32x4 o = {pk2(f[0] * rs, f[1] * rs), pk2(f[2] * rs, f[3] * rs), pk2(f[4] * rs, f[5] * rs), pk2(f[6] * rs, f[7] * rs)};
    *(u32x4*)(ws + WS_XN + ((size_t)row * DM + lane * 8) * 2) = o;
  }
  const int gt = blockIdx.x * 256 + t;
  if (gt < 65536) { u32x4 z = {0, 0, 0, 0}; *(u32x4*)(ws + WS_PART + (size_t)gt * 16) = z; }
  else if (gt < 65536 + 32768) {
    const int u = gt - 65536;
    u32x4 z = {~0u, ~0u, ~0u, ~0u};
    if (u < 512) {
      const int ks = u >> 6, ga = (u >> 4) & 3, hb = (u >> 3) & 1, i = u & 7;
#pragma unroll
      for (int a = 0; a < 4; ++a) z[a] = *(const unsigned*)(mem0 + i * DM + ks * 64 + ga * 16 + 4 * a + 2 * hb);
#pragma unroll
      for (int j = 0; j < 4; ++j) if (z[j] == ~0u) z[j] = 0x7fff7fffu;
    }
    *(u32x4*)(ws + WS_MBUF + (size_t)u * 16) = z;
  }
  else if (gt < 65536 + 32768 + 8192) { u32x4 z = {0, 0, 0, 0}; *(u32x4*)(ws + WS_CNT + (size_t)(gt - 65536 - 32768) * 16) = z; }
  asm volatile("" :: "v"(wa[0]), "v"(wa[1]), "v"(wa[2]), "v"(wa[3]), "v"(wa[4]), "v"(wa[5]), "v"(wa[6]), "v"(wa[7]));
}

constexpr int CL_W = 0, CL_PP = 131072  , CL_VX = CL_PP + 16384, CL_QK = CL_VX + 8192, CL_O = CL_QK + 4096, CL_R = CL_O + 2048, CL_END = CL_R + 2048;
#define DLY1 8
#define DLY2 13
constexpr unsigned SPIN_CAP = 1u << 18;
#define ACLOB "a0","a1","a2","a3","a4","a5","a6","a7","a8","a9","a10","a11","a12","a13","a14","a15","a16","a17","a18","a19","a20","a21","a22","a23","a24","a25","a26","a27","a28","a29","a30","a31","a32","a33","a34","a35","a36","a37","a38","a39","a40","a41","a42","a43","a44","a45","a46","a47","a48","a49","a50","a51","a52","a53","a54","a55","a56","a57","a58","a59","a60","a61","a62","a63","a64","a65","a66","a67","a68","a69","a70","a71","a72","a73","a74","a75","a76","a77","a78","a79","a80","a81","a82","a83","a84","a85","a86","a87","a88","a89","a90","a91","a92","a93","a94","a95","a96","a97","a98","a99","a100","a101","a102","a103","a104","a105","a106","a107","a108","a109","a110","a111","a112","a113","a114","a115","a116","a117","a118","a119","a120","a121","a122","a123","a124","a125","a126","a127","a128","a129","a130","a131","a132","a133","a134","a135","a136","a137","a138","a139","a140","a141","a142","a143","a144","a145","a146","a147","a148","a149","a150","a151","a152","a153","a154","a155","a156","a157","a158","a159","a160","a161","a162","a163","a164","a165","a166","a167","a168","a169","a170","a171","a172","a173","a174","a175","a176","a177","a178","a179","a180","a181","a182","a183","a184","a185","a186","a187","a188","a189","a190","a191","a192","a193","a194","a195","a196","a197","a198","a199","a200","a201","a202","a203","a204","a205","a206","a207","a208","a209","a210","a211","a212","a213","a214","a215","a216","a217","a218","a219","a220","a221","a222","a223","a224","a225","a226","a227","a228","a229","a230","a231","a232","a233","a234","a235","a236","a237","a238","a239","a240","a241","a242","a243","a244","a245","a246","a247","a248","a249","a250","a251","a252","a253","a254","a255"

DI int lane_id() { int l; asm volatile("v_mbcnt_lo_u32_b32 %0, -1, 0\n\tv_mbcnt_hi_u32_b32 %0, -1, %0" : "=v"(l)); return l; }
DI float max3f(float a, float b, float c) { float r; asm("v_max3_f32 %0, %1, %2, %3" : "=v"(r) : "v"(a), "v"(b), "v"(c)); return r; }
DI float max2f(float a, float b) { float r; asm("v_max_f32_e32 %0, %1, %2" : "=v"(r) : "v"(a), "v"(b)); return r; }
DI void st16_sc1(void* p, u32x4 v) { asm volatile("global_store_dwordx4 %0, %1, off sc1\n\ts_nop 1" ::"v"(p), "v"(v) : "memory"); }
DI void st4_sc1(void* p, unsigned v) { asm volatile("global_store_dword %0, %1, off sc1" ::"v"(p), "v"(v) : "memory"); }
DI void dma16(const void* src, unsigned dst) {
  asm volatile("s_mov_b32 m0, %1\n\ts_nop 0\n\tglobal_load_lds_dwordx4 %0, off" ::"v"(src), "s"(dst) : "memory", "m0");
}
template <int N, int OFF> DI void tr_to_acc(unsigned addr) {
  asm volatile("ds_read_b64_tr_b16 a[%c1:%c2], %0 offset:%c3" ::"v"(addr), "i"(N), "i"(N + 1), "i"(OFF) : "memory", ACLOB);
}
template <int A0, int BUF> DI void chunk_to_acc(unsigned addr) {
  tr_to_acc<A0 + 0, BUF + 0>(addr); tr_to_acc<A0 + 2, BUF + 256>(addr); tr_to_acc<A0 + 4, BUF + 2048>(addr); tr_to_acc<A0 + 6, BUF + 2304>(addr);
  tr_to_acc<A0 + 8, BUF + 32>(addr); tr_to_acc<A0 + 10, BUF + 288>(addr); tr_to_acc<A0 + 12, BUF + 2080>(addr); tr_to_acc<A0 + 14, BUF + 2336>(addr);
}
template <int KS> DI void qk_step(f32x4& q0, f32x4& q1, f32x4& k0, f32x4& k1, bf16x8 m, int sidx) {
  asm volatile("v_smfmac_f32_16x16x64_bf16 %0, %4, a[%c6:%c7], %5\n\t"
               "v_smfmac_f32_16x16x64_bf16 %1, %4, a[%c8:%c9], %5\n\t"
               "v_smfmac_f32_16x16x64_bf16 %2, %4, a[%c10:%c11], %5\n\t"
               "v_smfmac_f32_16x16x64_bf16 %3, %4, a[%c12:%c13], %5"
               : "+v"(q0), "+v"(q1), "+v"(k0), "+v"(k1)
               : "v"(m), "v"(sidx), "i"(KS * 32), "i"(KS * 32 + 7), "i"(KS * 32 + 8), "i"(KS * 32 + 15), "i"(KS * 32 + 16), "i"(KS * 32 + 23), "i"(KS * 32 + 24), "i"(KS * 32 + 31)
               : ACLOB);
}
DI void smf2(f32x4& d0, f32x4& d1, bf16x8 m, u32x8 w0, u32x8 w1, int sidx) {
  asm volatile("v_smfmac_f32_16x16x64_bf16 %0, %2, %3, %5\n\tv_smfmac_f32_16x16x64_bf16 %1, %2, %4, %5"
               : "+v"(d0), "+v"(d1) : "v"(m), "v"(w0), "v"(w1), "v"(sidx));
}
#define STAMP(i)

DI unsigned ld4_sc1(const void* p) { unsigned r; asm volatile("global_load_dword %0, %1, off sc1\n\ts_waitcnt vmcnt(0)" : "=&v"(r) : "v"(p) : "memory"); return r; }
DI void st8_sc1(void* p, u32x2 v) { asm volatile("global_store_dwordx2 %0, %1, off sc1\n\ts_nop 1" ::"v"(p), "v"(v) : "memory"); }

DI void chain_role(const bf16* __restrict__ Wq, const bf16* __restrict__ Wk, const bf16* __restrict__ Wv,
                   const bf16* __restrict__ Wo, char* ws, char* lds, const int wv) {
  asm volatile("; CHAIN_BEGIN");
  const int h = (blockIdx.x & 7) + 8 * (blockIdx.x >> 4), jh = (blockIdx.x >> 3) & 1, cu = 2 * h + jh;
  const int lane = lane_id(), w = wv, t = w * 64 + lane, c = lane & 15, g = lane >> 4;
  u32x8 wvr[4][2];
  const unsigned ldsbase = (unsigned)(size_t)(LAS char*)lds;
  char* const wl = lds + CL_W + w * 32768 + lane * 16;
  {
    const unsigned stg = ldsbase + CL_W + w * 32768;
    const size_t soff = (size_t)(lane >> 2) * HD + h * 128 + w * 32 + (lane & 3) * 8;
    auto issue = [&](const bf16* W, int ks, int buf) {
#pragma unroll
      for (int i = 0; i < 4; ++i) dma16(W + (size_t)(ks * 64 + i * 16) * HD + soff, stg + buf * 4096 + i * 1024);
    };
    const unsigned ta = stg + (8 * g + (c >> 2)) * 64 + (c & 3) * 8;
#define WAITV(n) asm volatile("s_waitcnt vmcnt(" #n ")" ::: "memory")
#define WAITL() asm volatile("s_waitcnt lgkmcnt(0)" ::: "memory")
    issue(Wq, 0, 0); issue(Wk, 0, 1); issue(Wq, 1, 2); issue(Wk, 1, 3); issue(Wq, 2, 4); issue(Wk, 2, 5); issue(Wq, 3, 6); issue(Wk, 3, 7);
    WAITV(24); chunk_to_acc<0, 0>(ta); chunk_to_acc<16, 4096>(ta); WAITL(); issue(Wq, 4, 0); issue(Wk, 4, 1);
    WAITV(24); chunk_to_acc<32, 8192>(ta); chunk_to_acc<48, 12288>(ta); WAITL(); issue(Wq, 5, 2); issue(Wk, 5, 3);
    WAITV(24); chunk_to_acc<64, 16384>(ta); chunk_to_acc<80, 20480>(ta); WAITL(); issue(Wq, 6, 4); issue(Wk, 6, 5);
    WAITV(24); chunk_to_acc<96, 24576>(ta); chunk_to_acc<112, 28672>(ta); WAITL(); issue(Wq, 7, 6); issue(Wk, 7, 7);
    WAITV(24); chunk_to_acc<128, 0>(ta); chunk_to_acc<144, 4096>(ta); WAITL(); issue(Wv, 0, 0); issue(Wv, 1, 1);
    WAITV(24); chunk_to_acc<160, 8192>(ta); chunk_to_acc<176, 12288>(ta); WAITL(); issue(Wv, 2, 2); issue(Wv, 3, 3);
    WAITV(24); chunk_to_acc<192, 16384>(ta); chunk_to_acc<208, 20480>(ta); WAITL(); issue(Wv, 4, 4); issue(Wv, 5, 5);
    WAITV(24); chunk_to_acc<224, 24576>(ta); chunk_to_acc<240, 28672>(ta); WAITL(); issue(Wv, 6, 6); issue(Wv, 7, 7);
    const char* tv = lds + CL_W + w * 32768 + (8 * g + (c >> 2)) * 64 + (c & 3) * 16;
    auto rdv = [&](int buf, u32x8 (&d)[2]) {
#pragma unroll
      for (int e = 0; e < 2; ++e)
#pragma unroll
        for (int j = 0; j < 4; ++j) { u32x2 r = trread(tv + buf * 4096 + (j >> 1) * 2048 + (j & 1) * 256 + e * 8); d[e][2 * j] = r[0]; d[e][2 * j + 1] = r[1]; }
    };
    WAITV(16);
#pragma unroll
    for (int ks = 0; ks < 4; ++ks) rdv(ks, wvr[ks]);
    u32x8 tmp[4][2];
    WAITV(0);
#pragma unroll
    for (int ks = 0; ks < 4; ++ks) rdv(4 + ks, tmp[ks]);
    asm volatile("s_waitcnt lgkmcnt(0)" : "+v"(tmp[0][0]), "+v"(tmp[0][1]), "+v"(tmp[1][0]), "+v"(tmp[1][1]), "+v"(tmp[2][0]), "+v"(tmp[2][1]), "+v"(tmp[3][0]), "+v"(tmp[3][1]),
                 "+v"(wvr[0][0]), "+v"(wvr[0][1]), "+v"(wvr[1][0]), "+v"(wvr[1][1]), "+v"(wvr[2][0]), "+v"(wvr[2][1]), "+v"(wvr[3][0]), "+v"(wvr[3][1]) :: "memory");
    {
      const bf16* src = Wo + (size_t)(h * 128 + (lane >> 3)) * DM + jh * 256 + w * 64 + (lane & 7) * 8;
#pragma unroll
      for (int i = 0; i < 16; ++i) dma16(src + (size_t)i * 8 * DM, stg + 16384 + i * 1024);
    }
#pragma unroll
    for (int ks = 0; ks < 4; ++ks)
#pragma unroll
      for (int e = 0; e < 2; ++e) {
        char* p0 = wl + ks * 4096 + e * 2048;
        *(u32x4*)p0 = (u32x4){tmp[ks][e][0], tmp[ks][e][1], tmp[ks][e][2], tmp[ks][e][3]};
        *(u32x4*)(p0 + 1024) = (u32x4){tmp[ks][e][4], tmp[ks][e][5], tmp[ks][e][6], tmp[ks][e][7]};
      }
    WAITV(0);
    const char* to = lds + CL_W + w * 32768 + 16384 + (8 * g + (c >> 2)) * 128 + (c & 3) * 32;
#pragma unroll
    for (int T = 0; T < 4; ++T)
#pragma unroll
      for (int ks = 0; ks < 2; ++ks)
#pragma unroll
        for (int j = 0; j < 4; ++j) { u32x2 r = trread(to + ks * 8192 + (j >> 1) * 4096 + (j & 1) * 512 + T * 8); tmp[T][ks][2 * j] = r[0]; tmp[T][ks][2 * j + 1] = r[1]; }
    asm volatile("s_waitcnt lgkmcnt(0)" : "+v"(tmp[0][0]), "+v"(tmp[0][1]), "+v"(tmp[1][0]), "+v"(tmp[1][1]), "+v"(tmp[2][0]), "+v"(tmp[2][1]), "+v"(tmp[3][0]), "+v"(tmp[3][1]) :: "memory");
#pragma unroll
    for (int T = 0; T < 4; ++T)
#pragma unroll
      for (int ks = 0; ks < 2; ++ks) {
        char* p0 = wl + 16384 + (T * 2 + ks) * 2048;
        *(u32x4*)p0 = (u32x4){tmp[T][ks][0], tmp[T][ks][1], tmp[T][ks][2], tmp[T][ks][3]};
        *(u32x4*)(p0 + 1024) = (u32x4){tmp[T][ks][4], tmp[T][ks][5], tmp[T][ks][6], tmp[T][ks][7]};
      }
  }
  const int sidx = (c & 8) ? 0xEEEE : 0x4444;
  const float sc = 0.08838834764831845f * 1.4426950408889634f;
  u32x8 wr1[8];
  const unsigned o_kx = (unsigned)(c * HD + h * 128 + w * 32 + 8 * g) * 2u;
  const unsigned o_vx = (unsigned)((h * DH + w * 32 + 8 * (c >> 2) + (c & 3)) * 32 + 8 * g) * 2u;
  const unsigned o_t16 = (unsigned)t * 16u;
  const unsigned o_pd = (unsigned)(((32 * jh + 8 * w + (c >> 1)) * 32 + h) * 256 + ((c & 1) * 4 + (lane >> 5) * 2 + (g & 1)) * 16);
  auto dmakv = [&](int sn) {
    const char* kb = ws + WS_K + (size_t)sn * 32 * HD * 2; const char* vb = ws + WS_VT + (size_t)sn * NH * DH * 32 * 2;
    const unsigned d0 = ldsbase + CL_PP + ((sn & 1) ^ 1) * 8192 + w * 1024, d1 = ldsbase + CL_VX + w * 1024;
    asm volatile("s_mov_b32 m0, %5\n\ts_nop 0\n\tglobal_load_lds_dwordx4 %0, %2 sc1\n\t"
                 "s_add_u32 m0, m0, 4096\n\ts_nop 0\n\tglobal_load_lds_dwordx4 %0, %3 sc1\n\t"
                 "s_mov_b32 m0, %6\n\ts_nop 0\n\tglobal_load_lds_dwordx4 %1, %4 sc1\n\t"
                 "s_add_u32 m0, m0, 3840\n\ts_nop 0\n\tglobal_load_lds_dwordx4 %1, %4 offset:256 sc1"
                 :: "v"(o_kx), "v"(o_vx), "s"(kb), "s"(kb + (size_t)16 * HD * 2), "s"(vb), "s"(d0), "s"(d1) : "memory", "scc", "m0");
  };
  auto ldw2 = [&](int off, int i) {
    u32x4 a = *(const u32x4*)(wl + off + i * 2048), b = *(const u32x4*)(wl + off + i * 2048 + 1024);
    wr1[i] = (u32x8){a[0], a[1], a[2], a[3], b[0], b[1], b[2], b[3]};
  };
  unsigned cap = SPIN_CAP;
  const char* const kvflag = ws + WS_CNT + (size_t)(C_KV + h) * 128;
  auto waitkv = [&](const char* p) {
    for (unsigned spins = 0; __builtin_amdgcn_readfirstlane(ld4_sc1(p)) < 4u; ++spins) {
      if (spins >= cap) { cap = 0; break; }
      __builtin_amdgcn_s_sleep(2);
    }
  };
  waitkv(ws + WS_CNT + (size_t)(C_E0 + h) * 128);
  dmakv(0);
#pragma unroll
  for (int i = 0; i < 8; ++i) ldw2(0, i);
  const unsigned ones2 = 0x3f803f80u;
  const bf16x8 onesx = mk8(ones2, ones2, ones2, ones2);
  const bf16x8 onesm = mk8(g < 2 ? ones2 : 0u, g < 2 ? ones2 : 0u, 0u, 0u);
  const unsigned tmask = ~127u;
  const unsigned zero_off = 0;
  auto step = [&](const int s, auto LASTC) {
    constexpr bool LAST = decltype(LASTC)::value;
    STAMP(0);
    unsigned kvv;
    char* const lm = lds + CL_PP + (s & 1) * 8192;
    const char* const lk = lds + CL_PP + ((s & 1) ^ 1) * 8192;
    f32x4 aq[2], ak[2], av[2];
#pragma unroll
    for (int i = 0; i < 2; ++i) { aq[i] = (f32x4){0, 0, 0, 0}; ak[i] = aq[i]; av[i] = aq[i]; }
    asm volatile("" : "+v"(aq[0]), "+v"(aq[1]), "+v"(ak[0]), "+v"(ak[1]), "+v"(av[0]), "+v"(av[1]));
    const char* fsrc = kvflag + (size_t)(LAST ? 0 : ((s + 1) >> 2)) * NH * 128;
    {
      const char* src = ws + WS_MBUF + (size_t)s * 8192;
      u32x4 a, b;
      for (unsigned spins = 0;; ++spins) {
        asm volatile("global_load_dwordx4 %0, %3, %4 sc1\n\tglobal_load_dwordx4 %1, %3, %5 sc1\n\tglobal_load_dword %2, %6, %7 sc1\n\ts_waitcnt vmcnt(0)"
                     : "=&v"(a), "=&v"(b), "=&v"(kvv) : "v"(o_t16), "s"(src), "s"(src + 4096), "v"(zero_off), "s"(fsrc) : "memory");
        *(u32x4*)(lm + t * 16) = a;
        *(u32x4*)(lm + 4096 + t * 16) = b;
        const unsigned mxu = max(max(max(a[0], a[1]), max(a[2], a[3])), max(max(b[0], b[1]), max(b[2], b[3])));
        if (__all(mxu != ~0u)) break;
        if (spins >= cap) { cap = 0; break; }
      }
    }
    STAMP(1);
    __syncthreads();
    bf16x8 mf[8];
#pragma unroll
    for (int ks = 0; ks < 8; ++ks) mf[ks] = *(const bf16x8*)(lm + ks * 1024 + lane * 16);
    qk_step<0>(aq[0], aq[1], ak[0], ak[1], mf[0], sidx); qk_step<1>(aq[0], aq[1], ak[0], ak[1], mf[1], sidx);
    qk_step<2>(aq[0], aq[1], ak[0], ak[1], mf[2], sidx); qk_step<3>(aq[0], aq[1], ak[0], ak[1], mf[3], sidx);
    qk_step<4>(aq[0], aq[1], ak[0], ak[1], mf[4], sidx); qk_step<5>(aq[0], aq[1], ak[0], ak[1], mf[5], sidx);
    qk_step<6>(aq[0], aq[1], ak[0], ak[1], mf[6], sidx); qk_step<7>(aq[0], aq[1], ak[0], ak[1], mf[7], sidx);
    smf2(av[0], av[1], mf[4], wr1[0], wr1[1], sidx);
    asm volatile("s_nop 6" : "+v"(aq[0]), "+v"(aq[1]), "+v"(ak[0]), "+v"(ak[1]));
    STAMP(2);
    {
      const int off = (w * 32 + 16 * (lane >> 5) + c) * 16 + (g & 1) * 8;
      u32x2 qq = {pk2(comb(aq[0][0], aq[1][0]), comb(aq[0][1], aq[1][1])), pk2(comb(aq[0][2], aq[1][2]), comb(aq[0][3], aq[1][3]))};
      u32x2 kk = {pk2(comb(ak[0][0], ak[1][0]), comb(ak[0][1], ak[1][1])), pk2(comb(ak[0][2], ak[1][2]), comb(ak[0][3], ak[1][3]))};
      *(u32x2*)(lds + CL_QK + off) = qq; *(u32x2*)(lds + CL_QK + 2048 + off) = kk;
    }
    __builtin_amdgcn_sched_barrier(0);
    smf2(av[0], av[1], mf[5], wr1[2], wr1[3], sidx);
    STAMP(3);
    __syncthreads();
    STAMP(4);
    bf16x8 qf[4], kf[4], kxf[2][4], vx[2];
    if constexpr (!LAST) {
      vx[0] = *(const bf16x8*)(lds + CL_VX + w * 1024 + lane * 16);
      vx[1] = *(const bf16x8*)(lds + CL_VX + (4 + w) * 1024 + lane * 16);
      __builtin_amdgcn_sched_barrier(0);
    }
    {
      const int off = (8 * g + (c >> 2)) * 16 + (c & 1) * 8;
#pragma unroll
      for (int ks = 0; ks < 4; ++ks) {
        u32x2 a = trread(lds + CL_QK + ks * 512 + off), b = trread(lds + CL_QK + ks * 512 + off + 64);
        qf[ks] = mk8(a[0], a[1], b[0], b[1]);
        a = trread(lds + CL_QK + 2048 + ks * 512 + off); b = trread(lds + CL_QK + 2048 + ks * 512 + off + 64);
        kf[ks] = mk8(a[0], a[1], b[0], b[1]);
        if constexpr (!LAST) {
          kxf[0][ks] = *(const bf16x8*)(lk + ks * 1024 + lane * 16);
          kxf[1][ks] = *(const bf16x8*)(lk + (4 + ks) * 1024 + lane * 16);
        }
        __builtin_amdgcn_sched_barrier(0);
        if (ks == 0) smf2(av[0], av[1], mf[6], wr1[4], wr1[5], sidx);
        if (ks == 1) smf2(av[0], av[1], mf[7], wr1[6], wr1[7], sidx);
        if (ks == 2) smf2(av[0], av[1], mf[0], wvr[0][0], wvr[0][1], sidx);
        if (ks == 3) smf2(av[0], av[1], mf[1], wvr[1][0], wvr[1][1], sidx);
      }
    }
    auto publish_kv = [&]() {
      if (jh == 0) {
        if (w == 0 && c < 8) {
          bf16* kd = (bf16*)(ws + WS_KM) + (size_t)(s * 8 + c) * HD + h * 128 + 8 * g;
#pragma unroll
          for (int ks = 0; ks < 4; ++ks) st16_sc1(kd + ks * 32, __builtin_bit_cast(u32x4, kf[ks]));
        }
      } else {
        u32x2 vv = {pk2(comb(av[0][0], av[1][0]), comb(av[0][1], av[1][1])), pk2(comb(av[0][2], av[1][2]), comb(av[0][3], av[1][3]))};
        st8_sc1((bf16*)(ws + WS_VMT) + (((size_t)s * NH + h) * DH + w * 32 + 8 * (c >> 2) + 4 * (lane >> 5) + (c & 3)) * 8 + (g & 1) * 4, vv);
      }
    };
    if constexpr (LAST) {
      smf2(av[0], av[1], mf[2], wvr[2][0], wvr[2][1], sidx);
      smf2(av[0], av[1], mf[3], wvr[3][0], wvr[3][1], sidx);
      asm volatile("s_nop 7" : "+v"(av[0]), "+v"(av[1]));
      publish_kv();
      asm volatile("s_waitcnt vmcnt(0)" ::: "memory");
      __syncthreads();
      if (t == 0) st4_sc1(ws + WS_CNT + (size_t)(C_CH + cu) * 128, (unsigned)(s + 1));
      return;
    }
    f32x4 sm[3];
    asm volatile("v_mfma_f32_16x16x32_bf16 %0, %3, %6, 0\n\tv_mfma_f32_16x16x32_bf16 %1, %4, %6, 0\n\tv_mfma_f32_16x16x32_bf16 %2, %5, %6, 0"
                 : "=&v"(sm[0]), "=&v"(sm[1]), "=&v"(sm[2]) : "v"(kf[0]), "v"(kxf[0][0]), "v"(kxf[1][0]), "v"(qf[0]));
    asm volatile("" : "+v"(vx[0]), "+v"(vx[1]));
    if (__builtin_expect(__builtin_amdgcn_readfirstlane(kvv) < 4u, 0)) waitkv(fsrc);
    dmakv(s + 1);
    ldw2(16384, 0);
    __builtin_amdgcn_sched_barrier(0);
#pragma unroll
    for (int ks = 1; ks < 4; ++ks) {
      asm volatile("v_mfma_f32_16x16x32_bf16 %0, %3, %6, %0\n\tv_mfma_f32_16x16x32_bf16 %1, %4, %6, %1\n\tv_mfma_f32_16x16x32_bf16 %2, %5, %6, %2"
                   : "+v"(sm[0]), "+v"(sm[1]), "+v"(sm[2]) : "v"(kf[ks]), "v"(kxf[0][ks]), "v"(kxf[1][ks]), "v"(qf[ks]));
      if (ks < 3) { ldw2(16384, 2 * ks - 1); ldw2(16384, 2 * ks); }
      __builtin_amdgcn_sched_barrier(0);
    }
    smf2(av[0], av[1], mf[2], wvr[2][0], wvr[2][1], sidx);
    asm volatile("s_nop 6" : "+v"(sm[0]), "+v"(sm[1]), "+v"(sm[2]));
    STAMP(10);
    float mx = max3f(max3f(sm[0][0], sm[0][1], sm[0][2]), max3f(sm[0][3], sm[1][0], sm[1][1]), max3f(max3f(sm[1][2], sm[1][3], sm[2][0]), max3f(sm[2][1], sm[2][2], sm[2][3]), sm[2][3]));
    smf2(av[0], av[1], mf[3], wvr[3][0], wvr[3][1], sidx);
    {
      auto r = __builtin_amdgcn_permlane16_swap(__float_as_uint(mx), __float_as_uint(mx), false, false);
      mx = max2f(__uint_as_float(r[0]), __uint_as_float(r[1]));
      r = __builtin_amdgcn_permlane32_swap(__float_as_uint(mx), __float_as_uint(mx), false, false);
      mx = max2f(__uint_as_float(r[0]), __uint_as_float(r[1]));
    }
    const float nm = -mx * sc;
#pragma unroll
    for (int j = 0; j < 3; ++j)
#pragma unroll
      for (int r = 0; r < 4; ++r) sm[j][r] = __builtin_amdgcn_exp2f(__builtin_fmaf(sm[j][r], sc, nm));
    asm volatile("" : "+v"(av[0]), "+v"(av[1]), "+v"(sm[0]), "+v"(sm[1]), "+v"(sm[2]));
    const bf16x8 px = mk8(pk2(sm[1][0], sm[1][1]), pk2(sm[1][2], sm[1][3]), pk2(sm[2][0], sm[2][1]), pk2(sm[2][2], sm[2][3]));
    const bf16x8 pm = mk8(pk2(sm[0][0], sm[0][1]), pk2(sm[0][2], sm[0][3]), 0u, 0u);
    const bf16x8 vm0 = mk8(pk2(av[0][0], av[0][1]), pk2(av[0][2], av[0][3]), 0u, 0u);
    const bf16x8 vm1 = mk8(pk2(av[1][0], av[1][1]), pk2(av[1][2], av[1][3]), 0u, 0u);
    STAMP(5);
    f32x4 d[4];
    {
      f32x4 o0, o1, rs;
      asm volatile("s_nop 1\n\tv_mfma_f32_16x16x32_bf16 %2, %9, %7, 0\n\tv_mfma_f32_16x16x32_bf16 %0, %3, %7, 0\n\tv_mfma_f32_16x16x32_bf16 %2, %10, %8, %2\n\t"
                   "v_mfma_f32_16x16x32_bf16 %1, %4, %7, 0\n\tv_mfma_f32_16x16x32_bf16 %0, %5, %8, %0\n\tv_mfma_f32_16x16x32_bf16 %1, %6, %8, %1"
                   : "=&v"(o0), "=&v"(o1), "=&v"(rs) : "v"(vx[0]), "v"(vx[1]), "v"(vm0), "v"(vm1), "v"(px), "v"(pm), "v"(onesx), "v"(onesm));
      ldw2(16384, 5); ldw2(16384, 6); ldw2(16384, 7);
      __builtin_amdgcn_sched_barrier(0);
      asm volatile("s_nop 1" : "+v"(rs));
      float inv = __builtin_amdgcn_rcpf(rs[0]);
#pragma unroll
      for (int T = 0; T < 4; ++T) d[T] = (f32x4){0, 0, 0, 0};
      asm volatile("s_nop 0" : "+v"(o0), "+v"(o1), "+v"(inv), "+v"(d[0]), "+v"(d[1]), "+v"(d[2]), "+v"(d[3]));
      const bool hb = c >= 8;
      const float e0 = hb ? o0[2] : o0[0], e1 = hb ? o0[3] : o0[1], e2 = hb ? o1[2] : o1[0], e3 = hb ? o1[3] : o1[1];
      u32x2 o = {pk2(e0 * inv, e1 * inv), pk2(e2 * inv, e3 * inv)};
      *(u32x2*)(lds + CL_O + (w >> 1) * 1024 + (16 * (2 * (w & 1) + (g >> 1)) + c) * 16 + (g & 1) * 8) = o;
    }
    __syncthreads();
    STAMP(6);
    const char* slot = ws + WS_PART + (size_t)(s & 1) * 524288;
    {
      const bf16x8 of0 = *(const bf16x8*)(lds + CL_O + lane * 16), of1 = *(const bf16x8*)(lds + CL_O + 1024 + lane * 16);
      smf2(d[0], d[1], of0, wr1[0], wr1[2], sidx); smf2(d[0], d[1], of1, wr1[1], wr1[3], sidx);
      smf2(d[2], d[3], of0, wr1[4], wr1[6], sidx); smf2(d[2], d[3], of1, wr1[5], wr1[7], sidx);
      asm volatile("s_nop 4" : "+v"(d[0]), "+v"(d[1]));
      const unsigned tag = (unsigned)(s + 1);
#pragma unroll
      for (int tp = 0; tp < 2; ++tp) {
        u32x4 v;
#pragma unroll
        for (int r = 0; r < 4; ++r) asm("v_and_or_b32 %0, %1, %2, %3" : "=v"(v[r]) : "v"(comb(d[2 * tp][r], d[2 * tp + 1][r])), "v"(tmask), "s"(tag));
        if (tp == 0) { asm volatile("global_store_dwordx4 %0, %1, %2 sc1\n\ts_nop 1" ::"v"(o_pd), "v"(v), "s"(slot) : "memory"); asm volatile("" : "+v"(d[2]), "+v"(d[3])); }
        else asm volatile("global_store_dwordx4 %0, %1, %2 offset:128 sc1\n\ts_nop 1" ::"v"(o_pd), "v"(v), "s"(slot) : "memory");
      }
    }
    STAMP(7);
    publish_kv();
#pragma unroll
    for (int i = 0; i < 8; ++i) ldw2(0, i);
#pragma unroll 1
    for (int k = 0; k < DLY1; ++k) __builtin_amdgcn_s_sleep(1);
    STAMP(11);
    {
      const char* src = slot + (size_t)cu * 8192;
      u32x4 r0, r1;
      const unsigned tag = (unsigned)(s + 1);
      for (unsigned spins = 0;; ++spins) {
        asm volatile("global_load_dwordx4 %0, %2, %3 sc1\n\tglobal_load_dwordx4 %1, %2, %4 sc1\n\ts_waitcnt vmcnt(0)"
                     : "=&v"(r0), "=&v"(r1) : "v"(o_t16), "s"(src), "s"(src + 4096) : "memory");
        float x0 = __uint_as_float(r0[0]) + __uint_as_float(r1[0]), x1 = __uint_as_float(r0[1]) + __uint_as_float(r1[1]);
        float x2 = __uint_as_float(r0[2]) + __uint_as_float(r1[2]), x3 = __uint_as_float(r0[3]) + __uint_as_float(r1[3]);
        const float y0 = comb(x0, x2), y1 = comb(x1, x3);
        auto rr = __builtin_amdgcn_permlane16_swap(__float_as_uint(y0), __float_as_uint(y1), false, false);
        const float z = __uint_as_float(rr[0]) + __uint_as_float(rr[1]);
        *(float*)(lds + CL_R + w * 256 + c * 16 + g * 4) = z;
        unsigned bad = 0;
#pragma unroll
        for (int j = 0; j < 4; ++j) bad |= (r0[j] ^ tag) | (r1[j] ^ tag);
        if (__all((bad & 127u) == 0)) break;
        if (spins >= cap) { cap = 0; break; }
      }
      STAMP(8);
      __syncthreads();
      if (t < 32) {
        const int i = t >> 2, cp = t & 3;
        const float* R = (const float*)(lds + CL_R) + ((cp >> 1) * 8 + (cp & 1) * 2 + (i >> 2)) * 4 + (i & 3);
        const float a = (R[0] + R[64]) + (R[128] + R[192]);
        const float b = (R[16] + R[80]) + (R[144] + R[208]);
        unsigned o = pk2(a, b);
        if (o == ~0u) o = 0x7fff7fffu;
        asm volatile("global_store_dword %0, %1, %2 sc1" ::"v"(o_t16 >> 2), "v"(o), "s"(ws + WS_MBUF + (size_t)(s + 1) * 8192 + cu * 128) : "memory");
      }
      if (t == 64) asm volatile("global_store_dword %0, %1, %2 sc1" ::"v"(zero_off), "v"(tag), "s"(ws + WS_CNT + (size_t)(C_CH + cu) * 128) : "memory");
      STAMP(9);
      const u32x4 zz = {0, 0, 0, 0};
      asm volatile("global_store_dwordx4 %0, %1, %2 sc1\n\tglobal_store_dwordx4 %0, %1, %3 sc1\n\ts_nop 1" ::"v"(o_t16), "v"(zz), "s"(src), "s"(src + 4096) : "memory");
    }
#pragma unroll 1
    for (int k = 0; k < DLY2; ++k) __builtin_amdgcn_s_sleep(1);
  };
#pragma unroll 1
  for (int s = 0; s < NSEG - 1; ++s) step(s, std::false_type{});
  step(NSEG - 1, std::true_type{});
}

DI void dma16_sc1(const void* src, unsigned dst) {
  asm volatile("s_mov_b32 m0, %1\n\ts_nop 0\n\tglobal_load_lds_dwordx4 %0, off sc1" ::"v"(src), "s"(dst) : "memory", "m0");
}
template <int N> DI void waitv() { asm volatile("s_waitcnt vmcnt(%c0)" ::"i"(N) : "memory"); }

#define TRACE(id, i)
constexpr unsigned BSPIN = 1u << 19;
#define SGAP 255
constexpr unsigned STEPGAP = SGAP;
#define PSLEEP 12
struct Pace { unsigned long long tlast; unsigned backlog; };
DI void wait_multi(const char* ws, int idx, unsigned target, Pace& pc, unsigned gap, const int wv, const bool fast = false) {
  if (wv == 0) {
    const char* p = ws + WS_CNT + (size_t)idx * 128;
    const unsigned long long nb = pc.tlast + (fast ? 0u : gap);
    unsigned long long now = __builtin_amdgcn_s_memrealtime();
    bool ok = false;
    if (pc.backlog) ok = __all(ld4_sc1(p) >= target);
    if (!ok) {
      pc.backlog = 0;
      for (unsigned spins = 0; now < nb && spins < 8192u; ++spins) { __builtin_amdgcn_s_sleep(4); now = __builtin_amdgcn_s_memrealtime(); }
      for (unsigned spins = 0; !__all(ld4_sc1(p) >= target) && spins < BSPIN; ++spins) {
        if (fast) __builtin_amdgcn_s_sleep(1); else __builtin_amdgcn_s_sleep(PSLEEP);
        now = __builtin_amdgcn_s_memrealtime();
      }
    }
    pc.tlast = now;
  }
  __syncthreads();
}
DI void publish_cnt(char* ws, int idx, const int wv, unsigned val = 1u) {
  asm volatile("s_waitcnt vmcnt(0)" ::: "memory");
  __syncthreads();
  if (wv == 0 && lane_id() == 0) __hip_atomic_fetch_add((unsigned*)(ws + WS_CNT + (size_t)idx * 128), val, __ATOMIC_RELAXED, __HIP_MEMORY_SCOPE_AGENT);
}
DI u32x4 pk8(f32x4 a, f32x4 b) { return (u32x4){pk2(a[0], a[1]), pk2(a[2], a[3]), pk2(b[0], b[1]), pk2(b[2], b[3])}; }
DI u32x4 bld16(__amdgpu_buffer_rsrc_t r, size_t off) { return __builtin_bit_cast(u32x4, __builtin_amdgcn_raw_buffer_load_b128(r, (unsigned)off, 0, 16)); }
DI u32x2 bld8(__amdgpu_buffer_rsrc_t r, size_t off) { return __builtin_bit_cast(u32x2, __builtin_amdgcn_raw_buffer_load_b64(r, (unsigned)off, 0, 16)); }
DI void bst16(__amdgpu_buffer_rsrc_t r, size_t off, u32x4 v) { __builtin_amdgcn_raw_buffer_store_b128(v, r, (unsigned)off, 0, 16); }
DI bf16x8 asb(u32x4 v) { return __builtin_bit_cast(bf16x8, v); }
DI float gelu_t(float x) {
  const float y = 0.7978845608028654f * (x + 0.044715f * x * x * x);
  return x * __builtin_amdgcn_rcpf(1.f + __builtin_amdgcn_exp2f(-2.f * 1.4426950408889634f * y));
}
DI float dppadd(float v, const int ctrl) {
  int r;
  switch (ctrl) {
    case 0: r = __builtin_amdgcn_update_dpp(0, __float_as_int(v), 0xB1, 0xf, 0xf, true); break;
    case 1: r = __builtin_amdgcn_update_dpp(0, __float_as_int(v), 0x4E, 0xf, 0xf, true); break;
    case 2: r = __builtin_amdgcn_update_dpp(0, __float_as_int(v), 0x141, 0xf, 0xf, true); break;
    default: r = __builtin_amdgcn_update_dpp(0, __float_as_int(v), 0x140, 0xf, 0xf, true); break;
  }
  return v + __int_as_float(r);
}
DI float wave_sum(float v) { v = dppadd(v, 0); v = dppadd(v, 1); v = dppadd(v, 2); v = dppadd(v, 3); return xsum32(xsum16(v)); }

template <int NT, int KS, class RowPtr>
DI void load_frags(bf16x8 (&wf)[NT][KS], RowPtr rowptr, char* stgp, const int colbyte[NT]) {
  const int lane = lane_id(), c = lane & 15, g = lane >> 4;
  constexpr int RB_ = NT * 32, RPP = 1024 / RB_, NP = KS * 32 / RPP;
  const unsigned stg = (unsigned)(size_t)(LAS char*)stgp;
#pragma unroll
  for (int i = 0; i < NP; ++i) dma16(rowptr(i * RPP + lane / (2 * NT), lane % (2 * NT)), stg + i * 1024);
  waitv<0>();
  const char* ta = stgp + (8 * g + (c >> 2)) * RB_;
#pragma unroll
  for (int n = 0; n < NT; ++n)
#pragma unroll
    for (int ks = 0; ks < KS; ++ks) {
      u32x2 lo = trread(ta + ks * 32 * RB_ + colbyte[n]), hi = trread(ta + (ks * 32 + 4) * RB_ + colbyte[n]);
      wf[n][ks] = mk8(lo[0], lo[1], hi[0], hi[1]);
    }
  asm volatile("s_waitcnt lgkmcnt(0)" ::: "memory");
}

template <int NT, int KS, int RS, int TMASK = 0>
DI void seg_gemm(const bf16x8 (&wf)[NT][KS], const char* img, f32x4 (&acc)[2][NT]) {
  const int lane = lane_id(), c = lane & 15, g = lane >> 4;
  constexpr int BT = 4, NB = KS / BT;
  static_assert(KS % BT == 0, "k-steps");
  const char* base = img + c * RS;
  bf16x8 af[2][BT][2];
  auto rd = [&](int b, bf16x8 (&d)[BT][2]) {
#pragma unroll
    for (int k = 0; k < BT; ++k)
#pragma unroll
      for (int mi = 0; mi < 2; ++mi) d[k][mi] = *(const bf16x8*)(base + mi * 16 * RS + b * 256 + (((k * 4 + g) ^ c) << 4));
  };
#pragma unroll
  for (int i = 0; i < 2; ++i)
#pragma unroll
    for (int n = 0; n < NT; ++n) acc[i][n] = (f32x4){0, 0, 0, 0};
  rd(0, af[0]);
#pragma unroll
  for (int b = 0; b < NB; ++b) {
    if (b + 1 < NB) rd(b + 1, af[(b + 1) & 1]);
    __builtin_amdgcn_sched_barrier(0);
#pragma unroll
    for (int k = 0; k < BT; ++k)
#pragma unroll
      for (int mi = 0; mi < 2; ++mi)
#pragma unroll
        for (int n = 0; n < NT; ++n)
          acc[mi][n] = ((TMASK >> n) & 1) ? mfma16(af[b & 1][k][mi], wf[n][b * BT + k], acc[mi][n]) : mfma16(wf[n][b * BT + k], af[b & 1][k][mi], acc[mi][n]);
    __builtin_amdgcn_sched_barrier(0);
  }
}

constexpr int NCHAIN = 64, NBULK = 192;
constexpr int N_AW = 64, N_FF = 2 * NFS, N_LN = 8, N_FIN = 16, N_SYS = N_AW + N_FF + N_LN + N_FIN, PJ0 = 144, N_PJ = NBULK - PJ0;
constexpr int T_PROJ = 48 * 32;

DI void stage_xn(const char* ws, int sg, unsigned dst, int w, int lane) {
#pragma unroll
  for (int i = 0; i < 8; ++i) {
    const int r = w * 8 + i;
    dma16(ws + WS_XN + ((size_t)(sg * 32 + r) * DM) * 2 + ((lane ^ (r & 15)) << 4), dst + r * 1024);
  }
}

DI void early_kv(int e, const bf16* __restrict__ Wk, const bf16* __restrict__ Wv, char* ws, char* lds, __amdgpu_buffer_rsrc_t R, const int wv) {
  const int lane = lane_id(), w = wv, c = lane & 15, g = lane >> 4;
  const int h = (e & 7) + 8 * ((e >> 3) & 3), isv = (e >> 5) & 1, half = e >> 6, d0 = half * 64 + w * 16;
  const unsigned ldsbase = (unsigned)(size_t)(LAS char*)lds;
  TRACE(e, 1);
  bf16x8 wf[1][16];
  {
    const int cb[1] = {(c & 3) * 8};
    const bf16* base = (isv ? Wv : Wk) + h * 128 + d0;
    stage_xn(ws, 0, ldsbase + 65536, w, lane); stage_xn(ws, 1, ldsbase + 98304, w, lane);
    load_frags<1, 16>(wf, [&](int r, int pc) { return base + (size_t)r * HD + pc * 8; }, lds + w * 16384, cb);
  }
  __syncthreads();
  stage_xn(ws, 2, ldsbase, w, lane); stage_xn(ws, 3, ldsbase + 32768, w, lane);
#pragma unroll 1
  for (int sg = 0; sg < 4; ++sg) {
    if (sg == 2) { waitv<0>(); __syncthreads(); }
    f32x4 acc[2][1];
    const char* img = lds + ((sg + 2) & 3) * 32768;
    if (isv) {
      seg_gemm<1, 16, 1024, 1>(wf, img, acc);
      bst16(R, WS_VT + ((((size_t)sg * NH + h) * DH + d0 + c) * 32 + g * 8) * 2, pk8(acc[0][0], acc[1][0]));
    } else {
      seg_gemm<1, 16, 1024, 0>(wf, img, acc);
#pragma unroll
      for (int mi = 0; mi < 2; ++mi)
        __builtin_amdgcn_raw_buffer_store_b64((u32x2){pk2(acc[mi][0][0], acc[mi][0][1]), pk2(acc[mi][0][2], acc[mi][0][3])}, R,
                                              (unsigned)(WS_K + ((size_t)(sg * 32 + mi * 16 + c) * HD + h * 128 + d0 + 4 * g) * 2), 0, 16);
    }
    if (sg == 0) publish_cnt(ws, C_E0 + h, wv);
  }
  publish_cnt(ws, C_KV + h, wv);
  TRACE(e, 2);
}

template <bool KV>
DI void pj_role(int id, const bf16* __restrict__ W0, const bf16* __restrict__ W1, char* ws, char* lds, __amdgpu_buffer_rsrc_t R, const int wv) {
  const int lane = lane_id(), w = wv, c = lane & 15, g = lane >> 4;
  const int h0 = KV ? id : (id & 7) + 16 * (id >> 3), h1 = KV ? id : h0 + 8;
  const unsigned ldsbase = (unsigned)(size_t)(LAS char*)lds;
  bf16x8 wf[4][16];
  {
    bf16x8 (&lo)[2][16] = *reinterpret_cast<bf16x8 (*)[2][16]>(&wf[0][0]);
    bf16x8 (&hi)[2][16] = *reinterpret_cast<bf16x8 (*)[2][16]>(&wf[2][0]);
    const int cp[2] = {(c & 3) * 16, (c & 3) * 16 + 8};
    const int cn[2] = {(c & 3) * 8, 32 + (c & 3) * 8};
    const bf16* b0 = W0 + h0 * 128 + w * 32; const bf16* b1 = W1 + h1 * 128 + w * 32;
    load_frags<2, 16>(lo, [&](int r, int pc) { return b0 + (size_t)r * HD + pc * 8; }, lds + w * 32768, cp);
    load_frags<2, 16>(hi, [&](int r, int pc) { return b1 + (size_t)r * HD + pc * 8; }, lds + w * 32768, KV ? cn : cp);
  }
  __syncthreads();
  constexpr int S0 = KV ? 4 : 0;
  auto pub = [&](int rb) {
    if (w == 3 && lane < 2) {
      if (KV) { if (lane == 0) __hip_atomic_fetch_add((unsigned*)(ws + WS_CNT + (size_t)(C_KV + rb * NH + h0) * 128), 4u, __ATOMIC_RELAXED, __HIP_MEMORY_SCOPE_AGENT); }
      else __hip_atomic_fetch_add((unsigned*)(ws + WS_CNT + (size_t)(C_Q + rb * 8 + ((lane ? h1 : h0) >> 2)) * 128), 1u, __ATOMIC_RELAXED, __HIP_MEMORY_SCOPE_AGENT);
    }
    TRACE(192 + rb * 64 + (KV ? id : 32 + id), 2);
  };
  stage_xn(ws, S0, ldsbase, w, lane); stage_xn(ws, S0 + 1, ldsbase + 32768, w, lane);
#pragma unroll 1
  for (int sg = S0; sg < NSEG; ++sg) {
    if (sg + 1 < NSEG) { if (sg > S0) waitv<12>(); else waitv<8>(); } else waitv<0>();
    __syncthreads();
    if ((sg & 3) == 1 && sg > S0 + 1) pub((sg >> 2) - 1);
    if (sg + 2 < NSEG) stage_xn(ws, sg + 2, ldsbase + ((sg + 2 - S0) % 3) * 32768, w, lane);
    f32x4 acc[2][4];
    seg_gemm<4, 16, 1024, KV ? 12 : 0>(wf, lds + ((sg - S0) % 3) * 32768, acc);
#pragma unroll
    for (int mi = 0; mi < 2; ++mi)
      bst16(R, (KV ? WS_K : WS_Q) + ((size_t)(sg * 32 + mi * 16 + c) * HD + h0 * 128 + w * 32 + 8 * g) * 2, pk8(acc[mi][0], acc[mi][1]));
    if (KV) {
#pragma unroll
      for (int n = 0; n < 2; ++n)
        bst16(R, WS_VT + ((((size_t)sg * NH + h1) * DH + w * 32 + n * 16 + c) * 32 + g * 8) * 2, pk8(acc[0][2 + n], acc[1][2 + n]));
    } else {
#pragma unroll
      for (int mi = 0; mi < 2; ++mi)
        bst16(R, WS_Q + ((size_t)(sg * 32 + mi * 16 + c) * HD + h1 * 128 + w * 32 + 8 * g) * 2, pk8(acc[mi][2], acc[mi][3]));
    }
  }
  asm volatile("s_waitcnt vmcnt(0)" ::: "memory");
  __syncthreads();
  pub(NRB - 1);
}

DI void aw_role(int id, const bf16* __restrict__ Wo, char* ws, char* lds, __amdgpu_buffer_rsrc_t R, const int wv) {
  const int lane = lane_id(), w = wv, t = w * 64 + lane, c = lane & 15, g = lane >> 4;
  const int par = id >> 4, hg = id & 7, nt = (id >> 3) & 1, h = hg * 4 + w;
  bf16x8 wf[4][16];
  {
    bf16x8 (&lo)[2][16] = *reinterpret_cast<bf16x8 (*)[2][16]>(&wf[0][0]);
    bf16x8 (&hi)[2][16] = *reinterpret_cast<bf16x8 (*)[2][16]>(&wf[2][0]);
    const int cb[2] = {(c & 3) * 8, 32 + (c & 3) * 8};
    const bf16* base = Wo + (size_t)hg * 512 * DM + nt * 256 + w * 64;
    load_frags<2, 16>(lo, [&](int r, int pc) { return base + (size_t)r * DM + pc * 8; }, lds + w * 32768, cb);
    load_frags<2, 16>(hi, [&](int r, int pc) { return base + (size_t)r * DM + 32 + pc * 8; }, lds + w * 32768, cb);
  }
  __syncthreads();
  const float sc = 0.08838834764831845f * 1.4426950408889634f;
  Pace tl0 = {0, 1}, tl1 = {0, 1};
#pragma unroll 1
  for (int s = par; s < NSEG; s += 4) {
    const int rb = s >> 2;
    TRACE(T_PROJ + s * 128 + (id & 15), 0);
    wait_multi(ws, lane < 4 ? C_KV + rb * NH + hg * 4 + lane : C_Q + rb * 8 + hg, 4u, tl0, 0u, wv);
    f32x4 st[3][2];
#pragma unroll
    for (int j = 0; j < 3; ++j) { st[j][0] = (f32x4){0, 0, 0, 0}; st[j][1] = st[j][0]; }
    bf16x8 qf[2][4]; u32x4 vxr[8];
    {
      bf16x8 kf[2][4];
#pragma unroll
      for (int ks = 0; ks < 4; ++ks) {
        const size_t col = h * 128 + ks * 32 + 8 * g;
#pragma unroll
        for (int x = 0; x < 2; ++x) {
          qf[x][ks] = asb(bld16(R, WS_Q + ((size_t)(s * 32 + 16 * x + c) * HD + col) * 2));
          kf[x][ks] = asb(bld16(R, WS_K + ((size_t)(s * 32 + 16 * x + c) * HD + col) * 2));
        }
      }
#pragma unroll
      for (int dt = 0; dt < 8; ++dt) {
        const size_t drow = ((size_t)s * NH + h) * DH + (dt >> 1) * 32 + 8 * (c >> 2) + 4 * (dt & 1) + (c & 3);
        vxr[dt] = bld16(R, WS_VT + (drow * 32 + 8 * g) * 2);
      }
#pragma unroll
      for (int ks = 0; ks < 4; ++ks) {
        st[1][0] = mfma16(kf[0][ks], qf[0][ks], st[1][0]); st[1][1] = mfma16(kf[0][ks], qf[1][ks], st[1][1]);
        st[2][1] = mfma16(kf[1][ks], qf[1][ks], st[2][1]);
      }
    }
    wait_multi(ws, C_CH + 8 * hg + (lane & 7), (unsigned)(s + 1), tl1, 4 * STEPGAP, wv, s >= NSEG - 1);
    TRACE(T_PROJ + s * 128 + (id & 15), 1);
    u32x2 vmr[8];
    {
      bf16x8 kmf[4];
#pragma unroll
      for (int ks = 0; ks < 4; ++ks) kmf[ks] = asb(bld16(R, WS_KM + ((size_t)(s * 8 + (c & 7)) * HD + h * 128 + ks * 32 + 8 * g) * 2));
#pragma unroll
      for (int dt = 0; dt < 8; ++dt) {
        const size_t drow = ((size_t)s * NH + h) * DH + (dt >> 1) * 32 + 8 * (c >> 2) + 4 * (dt & 1) + (c & 3);
        vmr[dt] = bld8(R, WS_VMT + (drow * 8 + (g & 1) * 4) * 2);
      }
#pragma unroll
      for (int ks = 0; ks < 4; ++ks) { st[0][0] = mfma16(kmf[ks], qf[0][ks], st[0][0]); st[0][1] = mfma16(kmf[ks], qf[1][ks], st[0][1]); }
    }
    bf16x8 px[2], pm[2];
#pragma unroll
    for (int qt = 0; qt < 2; ++qt) {
      float v[12];
#pragma unroll
      for (int r = 0; r < 4; ++r) {
        v[r] = (g < 2) ? st[0][qt][r] : -INFINITY;
        const int kj = 4 * g + r;
        v[4 + r] = (qt == 1 || kj <= c) ? st[1][qt][r] : -INFINITY;
        v[8 + r] = (qt == 1 && kj <= c) ? st[2][qt][r] : -INFINITY;
      }
      float mx = v[0];
#pragma unroll
      for (int i = 1; i < 12; ++i) mx = fmaxf(mx, v[i]);
      mx = xmax16(mx); mx = xmax32(mx);
      float sum = 0;
#pragma unroll
      for (int i = 0; i < 12; ++i) { v[i] = __builtin_amdgcn_exp2f((v[i] - mx) * sc); sum += v[i]; }
      sum = xsum16(sum); sum = xsum32(sum);
      const float inv = __builtin_amdgcn_rcpf(sum);
      px[qt] = mk8(pk2(v[4] * inv, v[5] * inv), pk2(v[6] * inv, v[7] * inv), pk2(v[8] * inv, v[9] * inv), pk2(v[10] * inv, v[11] * inv));
      pm[qt] = mk8(pk2(v[0] * inv, v[1] * inv), pk2(v[2] * inv, v[3] * inv), 0u, 0u);
    }
#pragma unroll
    for (int dj = 0; dj < 4; ++dj)
#pragma unroll
      for (int qt = 0; qt < 2; ++qt) {
        f32x4 o[2];
#pragma unroll
        for (int e = 0; e < 2; ++e) {
          o[e] = mfma16(asb(vxr[2 * dj + e]), px[qt], (f32x4){0, 0, 0, 0});
          o[e] = mfma16(mk8(vmr[2 * dj + e][0], vmr[2 * dj + e][1], 0u, 0u), pm[qt], o[e]);
        }
        *(u32x4*)(lds + (qt * 16 + c) * 1024 + (((w * 16 + dj * 4 + g) ^ c) << 4)) = pk8(o[0], o[1]);
      }
    __syncthreads();
    f32x4 acc[2][4];
    seg_gemm<4, 16, 1024>(wf, lds, acc);
#pragma unroll
    for (int mi = 0; mi < 2; ++mi)
#pragma unroll
      for (int n = 0; n < 4; ++n)
        bst16(R, WS_P + ((((size_t)s * SKW + hg) * 32 + mi * 16 + c) * DM + nt * 256 + w * 64 + n * 16 + 4 * g) * 4, __builtin_bit_cast(u32x4, acc[mi][n]));
    publish_cnt(ws, C_WO + s, wv);
    TRACE(T_PROJ + s * 128 + (id & 15), 2);
  }
}

DI void ln_role(int id, int par, const bf16* __restrict__ x, char* ws, __amdgpu_buffer_rsrc_t R, const int wv) {
  const int lane = lane_id(), w = wv;
  Pace tl = {0, 1};
#pragma unroll 1
  for (int s = par; s < NSEG; s += 2) {
    const int r32 = id * 4 + w, row = s * 32 + r32;
    const u32x4 v = *(const u32x4*)(x + (size_t)row * DM + lane * 8);
    wait_multi(ws, C_WO + s, 16u, tl, 2 * STEPGAP, wv, s >= NSEG - 1);
    TRACE(T_PROJ + s * 128 + 32 + id, 1);
    f32x4 p[SKW][2];
#pragma unroll
    for (int sk = 0; sk < SKW; ++sk) {
      const size_t o = WS_P + ((((size_t)s * SKW + sk) * 32 + r32) * DM + lane * 8) * 4;
      p[sk][0] = __builtin_bit_cast(f32x4, bld16(R, o)); p[sk][1] = __builtin_bit_cast(f32x4, bld16(R, o + 16));
    }
    f32x4 a0 = ((p[0][0] + p[1][0]) + (p[2][0] + p[3][0])) + ((p[4][0] + p[5][0]) + (p[6][0] + p[7][0]));
    f32x4 a1 = ((p[0][1] + p[1][1]) + (p[2][1] + p[3][1])) + ((p[4][1] + p[5][1]) + (p[6][1] + p[7][1]));
    const u32x4 ar = pk8(a0, a1);
    u32x4 hb;
#pragma unroll
    for (int i = 0; i < 4; ++i) hb[i] = pk2(bflo(v[i]) + bflo(ar[i]), bfhi(v[i]) + bfhi(ar[i]));
    float f[8] = {bflo(hb[0]), bfhi(hb[0]), bflo(hb[1]), bfhi(hb[1]), bflo(hb[2]), bfhi(hb[2]), bflo(hb[3]), bfhi(hb[3])};
    const float mean = wave_sum(((f[0] + f[1]) + (f[2] + f[3])) + ((f[4] + f[5]) + (f[6] + f[7]))) * (1.f / DM);
    float qq = 0;
#pragma unroll
    for (int i = 0; i < 8; ++i) { f[i] -= mean; qq += f[i] * f[i]; }
    const float rs = 1.f / sqrtf(wave_sum(qq) * (1.f / DM) + 1e-5f);
    u32x4 o = {pk2(f[0] * rs, f[1] * rs), pk2(f[2] * rs, f[3] * rs), pk2(f[4] * rs, f[5] * rs), pk2(f[6] * rs, f[7] * rs)};
    bst16(R, WS_HN + ((size_t)row * DM + lane * 8) * 2, o);
    bst16(R, WS_H + ((size_t)row * DM + lane * 8) * 2, hb);
    publish_cnt(ws, C_LN + s, wv);
    TRACE(T_PROJ + s * 128 + 32 + id, 2);
  }
}

DI void f1_role(int id, const bf16* __restrict__ Wg, const bf16* __restrict__ Wu, char* ws, char* lds, __amdgpu_buffer_rsrc_t R, const int wv) {
  const int lane = lane_id(), w = wv, t = w * 64 + lane, c = lane & 15, g = lane >> 4;
  const int par = id / NFS, fs = id % NFS;
  const int f0 = fs * 64 + w * 16;
  const bool valid = f0 < DF;
  bf16x8 wgu[2][16];
  {
    const int cb[2] = {(c & 3) * 8, 32 + (c & 3) * 8};
    const int fc = valid ? f0 : 0;
    load_frags<2, 16>(wgu, [&](int r, int pc) { return (pc < 2 ? Wg : Wu) + (size_t)r * DF + fc + (pc & 1) * 8; }, lds + w * 32768, cb);
  }
  __syncthreads();
  const unsigned ldsbase = (unsigned)(size_t)(LAS char*)lds;
  Pace tl = {0, 1};
#pragma unroll 1
  for (int s = par; s < NSEG; s += 2) {
    wait_multi(ws, C_LN + s, N_LN, tl, 2 * STEPGAP, wv, s >= NSEG - 2);
    TRACE(T_PROJ + s * 128 + 40 + fs, 1);
#pragma unroll
    for (int i = 0; i < 8; ++i) {
      const int r = w * 8 + i;
      dma16_sc1(ws + WS_HN + ((size_t)(s * 32 + r) * DM) * 2 + ((lane ^ (r & 15)) << 4), ldsbase + r * 1024);
    }
    waitv<0>();
    __syncthreads();
    f32x4 acc[2][2];
    seg_gemm<2, 16, 1024>(wgu, lds, acc);
#pragma unroll
    for (int mi = 0; mi < 2; ++mi) {
      float r[4];
#pragma unroll
      for (int j = 0; j < 4; ++j) r[j] = valid ? gelu_t(acc[mi][0][j]) * acc[mi][1][j] : 0.f;
      __builtin_amdgcn_raw_buffer_store_b64((u32x2){pk2(r[0], r[1]), pk2(r[2], r[3])}, R, (unsigned)(WS_ACT + ((size_t)(s * 32 + mi * 16 + c) * DFP + f0 + 4 * g) * 2), 0, 16);
    }
    publish_cnt(ws, C_FF + s, wv);
    TRACE(T_PROJ + s * 128 + 40 + fs, 2);
  }
}

DI void f2_role(int id, const bf16* __restrict__ Wd, char* ws, bf16* __restrict__ out, char* lds, __amdgpu_buffer_rsrc_t R, const int wv) {
  const int lane = lane_id(), w = wv, t = w * 64 + lane, c = lane & 15, g = lane >> 4;
  const int par = id >> 3, nb = id & 7, n0 = nb * 64 + w * 16;
  bf16x8 wd[1][44];
  {
    const int cb[1] = {(c & 3) * 8};
    bf16x8 (&lo)[1][22] = *reinterpret_cast<bf16x8 (*)[1][22]>(&wd[0][0]);
    bf16x8 (&hi)[1][22] = *reinterpret_cast<bf16x8 (*)[1][22]>(&wd[0][22]);
    load_frags<1, 22>(lo, [&](int r, int pc) { return Wd + (size_t)r * DM + n0 + pc * 8; }, lds + w * 32768, cb);
    load_frags<1, 22>(hi, [&](int r, int pc) { int fr = 704 + r; fr = fr < DF ? fr : DF - 1; return Wd + (size_t)fr * DM + n0 + pc * 8; }, lds + w * 32768, cb);
  }
  __syncthreads();
  const unsigned ldsbase = (unsigned)(size_t)(LAS char*)lds;
  unsigned soff[22];
#pragma unroll
  for (int i = 0; i < 22; ++i) {
    const int u = (w * 22 + i) * 64 + lane, r = u / 176, sl = u % 176;
    soff[i] = r * (DFP * 2) + (((sl & ~15) | ((sl ^ r) & 15)) << 4);
  }
  Pace tl = {0, 1};
#pragma unroll 1
  for (int s = par; s < NSEG; s += 2) {
    wait_multi(ws, C_FF + s, NFS, tl, 2 * STEPGAP, wv, s >= NSEG - 3);
    TRACE(T_PROJ + s * 128 + 64 + nb, 1);
    const char* src = ws + WS_ACT + (size_t)s * 32 * DFP * 2;
#pragma unroll
    for (int i = 0; i < 22; ++i) dma16_sc1(src + soff[i], ldsbase + (w * 22 + i) * 1024);
    u32x2 hv[2];
#pragma unroll
    for (int mi = 0; mi < 2; ++mi) hv[mi] = bld8(R, WS_H + ((size_t)(s * 32 + mi * 16 + c) * DM + n0 + 4 * g) * 2);
    waitv<0>();
    __syncthreads();
    f32x4 acc[2][1];
    seg_gemm<1, 44, DFP * 2>(wd, lds, acc);
#pragma unroll
    for (int mi = 0; mi < 2; ++mi) {
      const unsigned d0 = pk2(acc[mi][0][0], acc[mi][0][1]), d1 = pk2(acc[mi][0][2], acc[mi][0][3]);
      u32x2 o = {pk2(bflo(hv[mi][0]) + bflo(d0), bfhi(hv[mi][0]) + bfhi(d0)), pk2(bflo(hv[mi][1]) + bflo(d1), bfhi(hv[mi][1]) + bfhi(d1))};
      *(u32x2*)(out + (size_t)(s * 32 + mi * 16 + c) * DM + n0 + 4 * g) = o;
    }
    TRACE(T_PROJ + s * 128 + 64 + nb, 2);
  }
}

__global__ void __launch_bounds__(256, 1) mega_kernel(const bf16* __restrict__ x, const bf16* __restrict__ Wq, const bf16* __restrict__ Wk, const bf16* __restrict__ Wv,
                                                      const bf16* __restrict__ Wo, const bf16* __restrict__ Wg, const bf16* __restrict__ Wu, const bf16* __restrict__ Wd,
                                                      char* ws, bf16* __restrict__ out) {
  extern __shared__ __attribute__((aligned(16))) char lds[];
  const int wv = __builtin_amdgcn_readfirstlane(threadIdx.x >> 6);
  if (blockIdx.x < NCHAIN) { chain_role(Wq, Wk, Wv, Wo, ws, lds, wv); return; }
  const int b = blockIdx.x - NCHAIN;
  const __amdgpu_buffer_rsrc_t R = __builtin_amdgcn_make_buffer_rsrc(ws, 0, (int)WS_END, 0x00020000);
  if (b >= PJ0) {
    if (b < PJ0 + 32) pj_role<true>(b - PJ0, Wk, Wv, ws, lds, R, wv);
    else pj_role<false>(b - PJ0 - 32, Wq, Wq, ws, lds, R, wv);
    return;
  }
  if (b < 128) early_kv(b, Wk, Wv, ws, lds, R, wv);
#define EXPM 15
  const int xg = b & 7, slot = b >> 3;
  if (slot < 8) { if (EXPM & 1) aw_role(b, Wo, ws, lds, R, wv); }
  else if (slot < 16) {
    if ((xg & 3) == 0) { if (EXPM & 8) f2_role((xg >> 2) * 8 + (slot - 8), Wd, ws, out, lds, R, wv); }
    else {
      const int fs = ((xg & 3) - 1) * 8 + (slot - 8);
      if (fs < NFS && (EXPM & 4)) f1_role((xg >> 2) * NFS + fs, Wg, Wu, ws, lds, R, wv);
    }
  } else { if (EXPM & 2) ln_role(xg, slot - 16, x, ws, R, wv); }
}

extern "C" void kernel_launch(void* const* d_in, const int* in_sizes, int n_in, void* d_out, int out_size, void* d_ws, size_t ws_size,
                              hipStream_t stream) {
  const bf16* x = (const bf16*)d_in[0];
  const bf16* mem0 = (const bf16*)d_in[1];
  const bf16* Wq = (const bf16*)d_in[2];
  const bf16* Wk = (const bf16*)d_in[3];
  const bf16* Wv = (const bf16*)d_in[4];
  const bf16* Wo = (const bf16*)d_in[5];
  const bf16* Wg = (const bf16*)d_in[8];
  const bf16* Wu = (const bf16*)d_in[9];
  const bf16* Wd = (const bf16*)d_in[10];
  char* ws = (char*)d_ws;
  bf16* out = (bf16*)d_out;
  (void)hipFuncSetAttribute((const void*)mega_kernel, hipFuncAttributeMaxDynamicSharedMemorySize, CL_END);
  prep_kernel<<<512, 256, 0, stream>>>(x, mem0, ws, Wq, Wk, Wv, Wo);
  mega_kernel<<<NCHAIN + NBULK, 256, CL_END, stream>>>(x, Wq, Wk, Wv, Wo, Wg, Wu, Wd, ws, out);
}
```

```cpp
#include <hip/hip_runtime.h>
#include <hip/hip_bf16.h>
#include <stdint.h>
#include <type_traits>

typedef __bf16 bf16;
typedef __bf16 bf16x2 __attribute__((ext_vector_type(2)));
typedef __bf16 bf16x8 __attribute__((ext_vector_type(8)));
typedef short s16x4 __attribute__((ext_vector_type(4)));
typedef float f32x2 __attribute__((ext_vector_type(2)));
typedef float f32x4 __attribute__((ext_vector_type(4)));
typedef unsigned u32x2 __attribute__((ext_vector_type(2)));
typedef unsigned u32x4 __attribute__((ext_vector_type(4)));
typedef unsigned u32x8 __attribute__((ext_vector_type(8)));
#define LAS __attribute__((address_space(3)))
#define DI __device__ __forceinline__

constexpr int S_ = 2048, DM = 512, NH = 32, DH = 128, HD = NH * DH, MM = 8, SEG = 32, NSEG = S_ / SEG, DF = 1376, DFP = 1408;

constexpr int NRB = 16, SKW = 8, NFS = 22;
constexpr size_t WS_PART = 0;
constexpr size_t WS_MBUF = WS_PART + 2u * 524288u;
constexpr size_t WS_CNT  = WS_MBUF + 64u * 16384u;
constexpr size_t WS_VOTE = WS_CNT + 262144u;
constexpr size_t WS_XN   = WS_VOTE + 16384u;
constexpr size_t WS_Q    = WS_XN + (size_t)S_ * DM * 2;
constexpr size_t WS_K    = WS_Q + (size_t)S_ * HD * 2;
constexpr size_t WS_VT   = WS_K + (size_t)S_ * HD * 2;
constexpr size_t WS_KM   = WS_VT + (size_t)S_ * HD * 2;
constexpr size_t WS_P    = WS_KM + (size_t)NSEG * MM * HD * 4;
constexpr size_t WS_H    = WS_P + (size_t)SKW * S_ * DM * 4;
constexpr size_t WS_HN   = WS_H + (size_t)S_ * DM * 2;
constexpr size_t WS_ACT  = WS_HN + (size_t)S_ * DM * 2;
constexpr size_t WS_END  = WS_ACT + (size_t)NSEG * 98304;
constexpr int C_KV = 0, C_CH = C_KV + NRB * NH, C_Q = C_CH + 64, C_WO = C_Q + NRB * 8, C_LN = C_WO + NSEG, C_FF = C_LN + NSEG, C_E0 = C_FF + NSEG, C_NUM = C_E0 + 2 * NH;
static_assert(C_NUM <= 1024 && WS_Q % 16384 == 0 && WS_CNT % 16384 == 0, "layout");
static_assert(WS_END + (4u << 20) <= (256u << 20), "workspace");

DI unsigned nz(unsigned off, unsigned sp) { return off ^ ((sp ^ (off >> 8)) & 8192u); }
constexpr int PRING = 8, QRING = 32;
DI unsigned hside(int h) { return (unsigned)(h & 4) << 11; }
DI unsigned blk(size_t buf, int seg, int h, unsigned pol13) {
  return nz((unsigned)buf + (seg & (QRING - 1)) * 262144 + ((h & 3) | ((h >> 3) << 2)) * 16384, hside(h) ^ (seg < 4 ? 0u : pol13));
}
DI unsigned kmblk(int seg, int h, unsigned pol13) { return nz((unsigned)WS_KM + seg * 131072 + ((h & 1) | ((h >> 3) << 1)) * 16384, hside(h) ^ pol13) + (h & 2) * 2048; }
DI unsigned cnt(int idx, unsigned sp) { return nz((unsigned)WS_CNT + (idx >> 6) * 16384 + (idx & 63) * 128, sp); }
DI unsigned rowoff(size_t buf, int seg, int r32, unsigned pol13) { return nz((unsigned)buf + seg * 32768 + ((r32 >> 3) & 1) * 16384 + (r32 & 7) * 1024, (unsigned)(r32 >> 4 << 13) ^ pol13); }
DI unsigned actoff(int seg, int par, unsigned o, unsigned pol13) { return nz((unsigned)WS_ACT + seg * 98304 + (o >> 13) * 16384 + (o & 8191u), (unsigned)(par << 13) ^ pol13); }
DI unsigned pk2(float a, float b) { f32x2 f = {a, b}; return __builtin_bit_cast(unsigned, __builtin_convertvector(f, bf16x2)); }
DI float bflo(unsigned u) { return __uint_as_float(u << 16); }
DI float bfhi(unsigned u) { return __uint_as_float(u & 0xffff0000u); }
DI f32x4 mfma16(bf16x8 a, bf16x8 b, f32x4 c) { return __builtin_amdgcn_mfma_f32_16x16x32_bf16(a, b, c, 0, 0, 0); }
DI bf16x8 mk8(unsigned a, unsigned b, unsigned c, unsigned d) { u32x4 u = {a, b, c, d}; return __builtin_bit_cast(bf16x8, u); }
DI u32x2 trread(const char* lds_ptr) {
  s16x4 r = __builtin_amdgcn_ds_read_tr16_b64_v4i16((LAS s16x4*)lds_ptr);
  return __builtin_bit_cast(u32x2, r);
}
DI float xmax32(float x) { auto r = __builtin_amdgcn_permlane32_swap(__float_as_uint(x), __float_as_uint(x), false, false); return fmaxf(__uint_as_float(r[0]), __uint_as_float(r[1])); }
DI float xmax16(float x) { auto r = __builtin_amdgcn_permlane16_swap(__float_as_uint(x), __float_as_uint(x), false, false); return fmaxf(__uint_as_float(r[0]), __uint_as_float(r[1])); }
DI float xsum32(float x) { auto r = __builtin_amdgcn_permlane32_swap(__float_as_uint(x), __float_as_uint(x), false, false); return __uint_as_float(r[0]) + __uint_as_float(r[1]); }
DI float xsum16(float x) { auto r = __builtin_amdgcn_permlane16_swap(__float_as_uint(x), __float_as_uint(x), false, false); return __uint_as_float(r[0]) + __uint_as_float(r[1]); }
DI float comb(float a, float b) { auto r = __builtin_amdgcn_permlane32_swap(__float_as_uint(a), __float_as_uint(b), false, false); return __uint_as_float(r[0]) + __uint_as_float(r[1]); }

DI float dppadd(float v, const int ctrl) {
  int r;
  switch (ctrl) {
    case 0: r = __builtin_amdgcn_update_dpp(0, __float_as_int(v), 0xB1, 0xf, 0xf, true); break;
    case 1: r = __builtin_amdgcn_update_dpp(0, __float_as_int(v), 0x4E, 0xf, 0xf, true); break;
    case 2: r = __builtin_amdgcn_update_dpp(0, __float_as_int(v), 0x141, 0xf, 0xf, true); break;
    default: r = __builtin_amdgcn_update_dpp(0, __float_as_int(v), 0x140, 0xf, 0xf, true); break;
  }
  return v + __int_as_float(r);
}
DI float wave_sum(float v) { v = dppadd(v, 0); v = dppadd(v, 1); v = dppadd(v, 2); v = dppadd(v, 3); return xsum32(xsum16(v)); }

constexpr int NVOTE = 64, NVIT = 6;
DI char* al2m(char* p) { return p + ((0u - (unsigned)(size_t)p) & 2097151u); }
__global__ void __launch_bounds__(256) prep_kernel(const bf16* __restrict__ x, const bf16* __restrict__ mem0, char* ws) {
  const int t = threadIdx.x, lane = t & 63, w = t >> 6;
  ws = al2m(ws);
  const int bx = blockIdx.x;
  const int row = bx * 4 + w;
  {
    u32x4 v = *(const u32x4*)(x + (size_t)row * DM + lane * 8);
    float f[8] = {bflo(v[0]), bfhi(v[0]), bflo(v[1]), bfhi(v[1]), bflo(v[2]), bfhi(v[2]), bflo(v[3]), bfhi(v[3])};
    const float mean = wave_sum(((f[0] + f[1]) + (f[2] + f[3])) + ((f[4] + f[5]) + (f[6] + f[7]))) * (1.f / DM);
    float q = 0;
#pragma unroll
    for (int i = 0; i < 8; ++i) { f[i] -= mean; q += f[i] * f[i]; }
    const float rs = 1.f / sqrtf(wave_sum(q) * (1.f / DM) + 1e-5f);
    u32x4 o = {pk2(f[0] * rs, f[1] * rs), pk2(f[2] * rs, f[3] * rs), pk2(f[4] * rs, f[5] * rs), pk2(f[6] * rs, f[7] * rs)};
    *(u32x4*)(ws + WS_XN + ((size_t)row * DM + lane * 8) * 2) = o;
  }
  const int gt = bx * 256 + t;
  if (gt < 65536) { u32x4 z = {~0u, ~0u, ~0u, ~0u}; *(u32x4*)(ws + WS_PART + (size_t)gt * 16) = z; }
  else {
    const int u = gt - 65536;
    u32x4 z = {~0u, ~0u, ~0u, ~0u};
    if (u < 1024) {
#pragma unroll
      for (int a = 0; a < 4; ++a) z[a] = *(const unsigned*)(mem0 + (u & 7) * DM + ((u & 511) >> 3) * 8 + 4 * (a & 1) + 2 * (a >> 1));
#pragma unroll
      for (int j = 0; j < 4; ++j) if (z[j] == ~0u) z[j] = 0x7fff7fffu;
    }
    *(u32x4*)(ws + WS_MBUF + (size_t)u * 16) = z;
  }
  if (gt < 2048) *(unsigned*)(ws + WS_CNT + (size_t)gt * 128) = 0u;
  else if (gt < 2048 + NVOTE) *(unsigned*)(ws + WS_VOTE + (size_t)(gt - 2048) * 64) = 0u;
}

constexpr int CL_W = 0, CL_PP = 131072  , CL_VX = CL_PP + 16384, CL_QK = CL_VX + 8192, CL_O = CL_QK + 4096, CL_R = CL_O + 2048, CL_END = CL_R + 2048;
constexpr int DLY1 = 6, DLY2 = 6;
constexpr unsigned SPIN_CAP = 1u << 18;
#define ACLOB "a0","a1","a2","a3","a4","a5","a6","a7","a8","a9","a10","a11","a12","a13","a14","a15","a16","a17","a18","a19","a20","a21","a22","a23","a24","a25","a26","a27","a28","a29","a30","a31","a32","a33","a34","a35","a36","a37","a38","a39","a40","a41","a42","a43","a44","a45","a46","a47","a48","a49","a50","a51","a52","a53","a54","a55","a56","a57","a58","a59","a60","a61","a62","a63","a64","a65","a66","a67","a68","a69","a70","a71","a72","a73","a74","a75","a76","a77","a78","a79","a80","a81","a82","a83","a84","a85","a86","a87","a88","a89","a90","a91","a92","a93","a94","a95","a96","a97","a98","a99","a100","a101","a102","a103","a104","a105","a106","a107","a108","a109","a110","a111","a112","a113","a114","a115","a116","a117","a118","a119","a120","a121","a122","a123","a124","a125","a126","a127","a128","a129","a130","a131","a132","a133","a134","a135","a136","a137","a138","a139","a140","a141","a142","a143","a144","a145","a146","a147","a148","a149","a150","a151","a152","a153","a154","a155","a156","a157","a158","a159","a160","a161","a162","a163","a164","a165","a166","a167","a168","a169","a170","a171","a172","a173","a174","a175","a176","a177","a178","a179","a180","a181","a182","a183","a184","a185","a186","a187","a188","a189","a190","a191","a192","a193","a194","a195","a196","a197","a198","a199","a200","a201","a202","a203","a204","a205","a206","a207","a208","a209","a210","a211","a212","a213","a214","a215","a216","a217","a218","a219","a220","a221","a222","a223","a224","a225","a226","a227","a228","a229","a230","a231","a232","a233","a234","a235","a236","a237","a238","a239","a240","a241","a242","a243","a244","a245","a246","a247","a248","a249","a250","a251","a252","a253","a254","a255"

DI int lane_id() { int l; asm volatile("v_mbcnt_lo_u32_b32 %0, -1, 0\n\tv_mbcnt_hi_u32_b32 %0, -1, %0" : "=v"(l)); return l; }
DI void dma16(const void* src, unsigned dst) {
  asm volatile("s_mov_b32 m0, %1\n\ts_nop 0\n\tglobal_load_lds_dwordx4 %0, off" ::"v"(src), "s"(dst) : "memory", "m0");
}
template <int N, int OFF> DI void tr_to_acc(unsigned addr) {
  asm volatile("ds_read_b64_tr_b16 a[%c1:%c2], %0 offset:%c3" ::"v"(addr), "i"(N), "i"(N + 1), "i"(OFF) : "memory", ACLOB);
}
template <int A0, int BUF> DI void chunk_to_acc(const unsigned (&ad)[4]) {
  tr_to_acc<A0 + 0, BUF>(ad[0]); tr_to_acc<A0 + 2, BUF>(ad[1]); tr_to_acc<A0 + 4, BUF + 8192>(ad[0]); tr_to_acc<A0 + 6, BUF + 8192>(ad[1]);
  tr_to_acc<A0 + 8, BUF>(ad[2]); tr_to_acc<A0 + 10, BUF>(ad[3]); tr_to_acc<A0 + 12, BUF + 8192>(ad[2]); tr_to_acc<A0 + 14, BUF + 8192>(ad[3]);
}
template <int KS> DI void qk_step(f32x4& q0, f32x4& q1, f32x4& k0, f32x4& k1, bf16x8 m, int sidx) {
  asm volatile("v_smfmac_f32_16x16x64_bf16 %0, %4, a[%c6:%c7], %5\n\t"
               "v_smfmac_f32_16x16x64_bf16 %1, %4, a[%c8:%c9], %5\n\t"
               "v_smfmac_f32_16x16x64_bf16 %2, %4, a[%c10:%c11], %5\n\t"
               "v_smfmac_f32_16x16x64_bf16 %3, %4, a[%c12:%c13], %5"
               : "+v"(q0), "+v"(q1), "+v"(k0), "+v"(k1)
               : "v"(m), "v"(sidx), "i"(KS * 32), "i"(KS * 32 + 7), "i"(KS * 32 + 8), "i"(KS * 32 + 15), "i"(KS * 32 + 16), "i"(KS * 32 + 23), "i"(KS * 32 + 24), "i"(KS * 32 + 31)
               : ACLOB);
}
DI void smf2(f32x4& d0, f32x4& d1, bf16x8 m, u32x8 w0, u32x8 w1, int sidx) {
  asm volatile("v_smfmac_f32_16x16x64_bf16 %0, %2, %3, %5\n\tv_smfmac_f32_16x16x64_bf16 %1, %2, %4, %5"
               : "+v"(d0), "+v"(d1) : "v"(m), "v"(w0), "v"(w1), "v"(sidx));
}

template <int A0> DI void smfa(f32x4& d, bf16x8 m, int sidx) {
  asm volatile("v_smfmac_f32_16x16x64_bf16 %0, %1, a[%c3:%c4], %2" : "+v"(d) : "v"(m), "v"(sidx), "i"(A0), "i"(A0 + 7) : ACLOB);
}
DI void mf32z(f32x4& d, bf16x8 a, bf16x8 b) { asm volatile("v_mfma_f32_16x16x32_bf16 %0, %1, %2, 0" : "=&v"(d) : "v"(a), "v"(b)); }
DI void mf32(f32x4& d, bf16x8 a, bf16x8 b) { asm volatile("v_mfma_f32_16x16x32_bf16 %0, %1, %2, %0" : "+v"(d) : "v"(a), "v"(b)); }
DI f32x2 hsum(f32x4 d) { return (f32x2){d[0], d[1]} + (f32x2){d[2], d[3]}; }
DI unsigned pkh(f32x4 d) { return __builtin_bit_cast(unsigned, __builtin_convertvector(hsum(d), bf16x2)); }
DI void smf1(f32x4& d0, bf16x8 m, u32x8 w0, int sidx) {
  asm volatile("v_smfmac_f32_16x16x64_bf16 %0, %1, %2, %3" : "+v"(d0) : "v"(m), "v"(w0), "v"(sidx));
}
DI u32x2 trread_a(unsigned a) { return __builtin_bit_cast(u32x2, __builtin_amdgcn_ds_read_tr16_b64_v4i16((LAS s16x4*)(size_t)a)); }
#define SB() __builtin_amdgcn_sched_barrier(0)
DI unsigned ld4_sc1(const void* p) { unsigned r; asm volatile("global_load_dword %0, %1, off sc1\n\ts_waitcnt vmcnt(0)" : "=&v"(r) : "v"(p) : "memory"); return r; }
DI unsigned get_pol(const char* ws, unsigned& cap, const char* flag = nullptr) {
  unsigned vt = 1u;
  const char* vp = ws + WS_VOTE + lane_id() * 64;
  for (unsigned spins = 0;; ++spins) {
    unsigned fl = 4u;
    if (flag) asm volatile("global_load_dword %0, %2, off sc1\n\tglobal_load_dword %1, %3, off sc1\n\ts_waitcnt vmcnt(0)" : "=&v"(vt), "=&v"(fl) : "v"(vp), "v"(flag) : "memory");
    else vt = ld4_sc1(vp);
    if (__all(vt != 0u && fl >= 4u)) break;
    if (spins >= cap) { cap = 0; break; }
    __builtin_amdgcn_s_sleep(2);
  }
  return __builtin_amdgcn_readfirstlane(wave_sum(__uint_as_float(vt)) > 0.f ? 8192u : 0u);
}

DI void chain_role(const bf16* __restrict__ Wq, const bf16* __restrict__ Wk, const bf16* __restrict__ Wv,
                   const bf16* __restrict__ Wo, char* ws, char* lds, const int wv) {
  asm volatile("; CHAIN_BEGIN");
  const int h = (blockIdx.x & 7) + 8 * (blockIdx.x >> 4), jh = (blockIdx.x >> 3) & 1, cu = 2 * h + jh;
  const int lane = lane_id(), w = wv, t = w * 64 + lane, c = lane & 15, g = lane >> 4;
  const int side = (h >> 2) & 1, rq = 2 * (((h >> 3) << 4) | ((h & 3) << 2) | (jh << 1) | side);
  u32x8 wvr[4][2];
  const unsigned ldsbase = (unsigned)(size_t)(LAS char*)lds;
  char* const wl = lds + CL_W + w * 32768 + lane * 16;
  {
    const size_t srow = (size_t)(16 * w + g) * HD + h * 128;
    auto issue = [&](const bf16* W, int ks, int buf) {
#pragma unroll
      for (int i = 0; i < 4; ++i) dma16(W + srow + (size_t)(ks * 64 + i * 4) * HD + ((c ^ ((g << 2) | i)) << 3), ldsbase + buf * 16384 + (w * 4 + i) * 1024);
    };
    auto issue_o = [&](int ck, int buf) {
#pragma unroll
      for (int i = 0; i < 4; ++i) {
        const int r = 8 * w + 2 * i + (lane >> 5), sl = lane & 31;
        dma16(Wo + (size_t)(h * 128 + ck * 32 + r) * DM + jh * 256 + ((sl ^ (((r & 3) << 2) | ((r >> 2) & 3))) << 3), ldsbase + buf * 16384 + (w * 4 + i) * 1024);
      }
    };
    const int q4 = c >> 2, p4 = c & 3;
    unsigned aqk[4], lo64[4];
#pragma unroll
    for (int e = 0; e < 4; ++e) {
      const int tl = e >> 1, jj = e & 1, row = 8 * g + 4 * jj + q4, fx = (q4 << 2) | ((2 * g + jj) & 3);
      aqk[e] = ldsbase + row * 256 + (((4 * w + 2 * tl + (p4 >> 1)) ^ fx) << 4) + (p4 & 1) * 8;
      lo64[e] = row * 256 + (((4 * w + p4) ^ fx) << 4) + tl * 8;
    }
#define WAITV(n) asm volatile("s_waitcnt vmcnt(" #n ")" ::: "memory")
#define WAITL() asm volatile("s_waitcnt lgkmcnt(0)" ::: "memory")
#define BARR() asm volatile("s_barrier" ::: "memory")
    unsigned ahi[4] = {aqk[0] + 65536, aqk[1] + 65536, aqk[2] + 65536, aqk[3] + 65536};
    u32x8 tv[4][2], to[4][2];
    auto rdv = [&](int buf, u32x8 (&d)[2]) {
#pragma unroll
      for (int e = 0; e < 2; ++e)
#pragma unroll
        for (int j = 0; j < 4; ++j) { u32x2 r = trread(lds + buf * 16384 + (j >> 1) * 8192 + lo64[2 * e + (j & 1)]); d[e][2 * j] = r[0]; d[e][2 * j + 1] = r[1]; }
    };
    auto rdo = [&](int ks, int buf) {
#pragma unroll
      for (int T = 0; T < 4; ++T)
#pragma unroll
        for (int j = 0; j < 4; ++j) {
          const int row = 8 * g + 4 * (j & 1) + q4, fx = (q4 << 2) | ((2 * g + (j & 1)) & 3);
          u32x2 r = trread(lds + (buf + (j >> 1)) * 16384 + row * 512 + (((8 * w + 4 * (T >> 1) + p4) ^ fx) << 4) + (T & 1) * 8);
          to[T][ks][2 * j] = r[0]; to[T][ks][2 * j + 1] = r[1];
        }
    };
    issue(Wq, 0, 0); issue(Wk, 0, 1); issue(Wq, 1, 2); issue(Wk, 1, 3); issue(Wq, 2, 4); issue(Wk, 2, 5);
    WAITV(16); BARR(); issue(Wq, 3, 6); issue(Wk, 3, 7); chunk_to_acc<0, 0>(aqk); chunk_to_acc<16, 16384>(aqk); WAITL();
    WAITV(16); BARR(); issue(Wq, 4, 0); issue(Wk, 4, 1); chunk_to_acc<32, 32768>(aqk); chunk_to_acc<48, 49152>(aqk); WAITL();
    WAITV(16); BARR(); issue(Wq, 5, 2); issue(Wk, 5, 3); chunk_to_acc<64, 0>(ahi); chunk_to_acc<80, 16384>(ahi); WAITL();
    WAITV(16); BARR(); issue(Wq, 6, 4); issue(Wk, 6, 5); chunk_to_acc<96, 32768>(ahi); chunk_to_acc<112, 49152>(ahi); WAITL();
    WAITV(16); BARR(); issue(Wq, 7, 6); issue(Wk, 7, 7); chunk_to_acc<128, 0>(aqk); chunk_to_acc<144, 16384>(aqk); WAITL();
    WAITV(16); BARR(); issue(Wv, 0, 0); issue(Wv, 1, 1); chunk_to_acc<160, 32768>(aqk); chunk_to_acc<176, 49152>(aqk); WAITL();
    WAITV(16); BARR(); issue(Wv, 2, 2); issue(Wv, 3, 3); chunk_to_acc<192, 0>(ahi); chunk_to_acc<208, 16384>(ahi); WAITL();
    WAITV(16); BARR(); issue(Wv, 4, 4); issue(Wv, 5, 5); chunk_to_acc<224, 32768>(ahi); chunk_to_acc<240, 49152>(ahi); WAITL();
#define PINV(a) asm volatile("s_waitcnt lgkmcnt(0)" : "+v"(a[0][0]), "+v"(a[0][1]), "+v"(a[1][0]), "+v"(a[1][1]) :: "memory")
    WAITV(16); BARR(); issue(Wv, 6, 6); issue(Wv, 7, 7); rdv(0, wvr[0]); rdv(1, wvr[1]); PINV(wvr);
    WAITV(16); BARR(); issue_o(0, 0); issue_o(1, 1); rdv(2, wvr[2]); rdv(3, wvr[3]); PINV((wvr + 2));
    WAITV(16); BARR(); issue_o(2, 2); issue_o(3, 3); rdv(4, tv[0]); rdv(5, tv[1]); PINV(tv);
    WAITV(16); BARR(); rdv(6, tv[2]); rdv(7, tv[3]); PINV((tv + 2));
    WAITV(8); BARR(); rdo(0, 0);
    WAITV(0); BARR(); rdo(1, 2);
    asm volatile("s_waitcnt lgkmcnt(0)" : "+v"(to[0][0]), "+v"(to[0][1]), "+v"(to[1][0]), "+v"(to[1][1]), "+v"(to[2][0]), "+v"(to[2][1]), "+v"(to[3][0]), "+v"(to[3][1]) :: "memory");
    BARR();
#pragma unroll
    for (int k = 0; k < 4; ++k)
#pragma unroll
      for (int e = 0; e < 2; ++e) {
        char* p0 = wl + k * 4096 + e * 2048;
        *(u32x4*)p0 = (u32x4){tv[k][e][0], tv[k][e][1], tv[k][e][2], tv[k][e][3]};
        *(u32x4*)(p0 + 1024) = (u32x4){tv[k][e][4], tv[k][e][5], tv[k][e][6], tv[k][e][7]};
        p0 += 16384;
        *(u32x4*)p0 = (u32x4){to[k][e][0], to[k][e][1], to[k][e][2], to[k][e][3]};
        *(u32x4*)(p0 + 1024) = (u32x4){to[k][e][4], to[k][e][5], to[k][e][6], to[k][e][7]};
      }
  }
  unsigned cap = SPIN_CAP;
  const unsigned pol13 = get_pol(ws, cap, ws + cnt(C_E0 + h, hside(h)));
  const int sidx = (c & 2) ? 0xEEEE : 0x4444;
  const float sc = 0.08838834764831845f * 1.4426950408889634f;
  u32x8 wr1[8];
  const unsigned o_t16 = (unsigned)t * 16u;
  const unsigned o_pd = (unsigned)((((32 * jh + 8 * w + (c >> 2)) * 2 + (g >> 1)) * 32 + h) * 128 + ((g & 1) * 4 + (c & 3)) * 16) ^ pol13;
  unsigned o_l16 = (unsigned)lane * 16u;
  const int l_m = (w * 64 + g * 16 + 4 * ((c & 7) >> 1) + (c & 1)) * 16 + (c >> 3) * 8;
  const unsigned o_pub = (unsigned)((2 * ((c >> 2) & 1) + (g & 1)) * 16 + ((c >> 1) & 1) * 8 + (g >> 1) * 4 + (c >> 3) * 8192);
  const unsigned mnear = (unsigned)(side * 8192) ^ pol13;
#define SETM0(v) asm volatile("s_mov_b32 m0, %0" :: "s"(v) : "m0")
#define DMA(vo, base) asm volatile("global_load_lds_dwordx4 %0, %1 sc1" :: "v"(vo), "s"(base) : "memory", "m0")
  auto ldw1 = [&](int off, int i, int hf) {
    const u32x4 a = *(const u32x4*)(wl + off + i * 2048 + hf * 1024);
    wr1[i][4 * hf] = a[0]; wr1[i][4 * hf + 1] = a[1]; wr1[i][4 * hf + 2] = a[2]; wr1[i][4 * hf + 3] = a[3];
    SB();
  };
  auto ldw2 = [&](int off, int i) {
    u32x4 a = *(const u32x4*)(wl + off + i * 2048), b = *(const u32x4*)(wl + off + i * 2048 + 1024);
    wr1[i] = (u32x8){a[0], a[1], a[2], a[3], b[0], b[1], b[2], b[3]};
  };
  const char* const chflag = ws + cnt(C_CH + cu, mnear);
  auto waitkv = [&](const char* p) {
    for (unsigned spins = 0; __builtin_amdgcn_readfirstlane(ld4_sc1(p)) < 4u; ++spins) {
      if (spins >= cap) { cap = 0; break; }
      __builtin_amdgcn_s_sleep(2);
    }
  };
  {
    const unsigned d0 = ldsbase + CL_PP + 8192 + w * 1024, d1 = ldsbase + CL_VX + w * 1024;
    const char* kb = ws + blk(WS_K, 0, h, 0u) + w * 1024; const char* vb = ws + blk(WS_VT, 0, h, 0u) + w * 2048;
    SETM0(d0); asm volatile("s_nop 0"); DMA(o_l16, kb); SETM0(d0 + 4096); asm volatile("s_nop 0"); DMA(o_l16, kb + 4096);
    SETM0(d1); asm volatile("s_nop 0"); DMA(o_l16, vb); SETM0(d1 + 4096); asm volatile("s_nop 0"); DMA(o_l16, vb + 1024);
  }

#pragma unroll
  for (int i = 0; i < 8; ++i) ldw2(0, i);
  const unsigned zero_off = 0;
  const int lane0 = lane;
  unsigned a_qw = ldsbase + CL_QK + (w * 32 + c) * 32 + g * 8, a_qr = ldsbase + CL_QK + (8 * g + (c >> 2)) * 32 + (c & 3) * 8;
  asm volatile("" : "+v"(a_qw), "+v"(a_qr));
  const unsigned o_kv = jh == 0 ? (unsigned)((2 * (c >> 2) + (c & 1)) * 256 + 16 * g)
                                : (unsigned)(2048 + (w * 32 + 8 * (c >> 2) + (c & 3)) * 16 + 4 * g);
  unsigned a_ow = ldsbase + ((c & 2) ? CL_O + (w >> 1) * 1024 + (16 * (2 * (w & 1) + (g >> 1)) + c - 2) * 16 + (g & 1) * 8 : CL_R + (t & 127) * 8);
  unsigned a_or = ldsbase + CL_O + lane * 16;
  asm volatile("" : "+v"(a_ow), "+v"(a_or));
  auto step = [&](const int s, auto LASTC) {
    constexpr bool LAST = decltype(LASTC)::value;
    const int lane = LAST ? lane_id() : lane0, t = w * 64 + lane, c = lane & 15, g = lane >> 4;
    unsigned a_mw = ldsbase + CL_PP + (s & 1) * 8192 + l_m, a_mw2 = a_mw + 4096;
    unsigned a_mr = ldsbase + CL_PP + (s & 1) * 8192 + lane * 16, a_kr = ldsbase + CL_PP + ((s & 1) ^ 1) * 8192 + lane * 16;
    const char* slot = ws + WS_PART + (size_t)(s & 1) * 524288;
    const char* slot2 = slot + 32768;
    const char* kb = ws + blk(WS_K, s + 1, h, pol13) + w * 1024; const char* kb2 = kb + 4096;
    const char* vb = ws + blk(WS_VT, s + 1, h, pol13) + w * 2048; const char* vb2 = vb + 1024;
    unsigned dk = ldsbase + CL_PP + (s & 1) * 8192 + w * 1024, dk2 = dk + 4096;
    const unsigned dv = ldsbase + CL_VX + w * 1024;
    asm volatile("" : "+s"(slot), "+s"(slot2), "+v"(a_mw), "+v"(a_mw2), "+v"(a_mr), "+v"(a_kr));
    if constexpr (!LAST) asm volatile("" : "+s"(kb), "+s"(kb2), "+s"(vb), "+s"(vb2), "+s"(dk), "+s"(dk2));
    f32x4 aq[2], ak[2], av[2];
#pragma unroll
    for (int i = 0; i < 2; ++i) { aq[i] = (f32x4){0, 0, 0, 0}; ak[i] = aq[i]; av[i] = aq[i]; }
    asm volatile("" : "+v"(aq[0]), "+v"(aq[1]), "+v"(ak[0]), "+v"(ak[1]), "+v"(av[0]), "+v"(av[1]));
    {
      const char* src = ws + WS_MBUF + (size_t)s * 16384 + mnear;
      u32x4 a, b;
      for (unsigned spins = 0;; ++spins) {
        asm volatile("global_load_dwordx4 %0, %2, %3 sc1\n\tglobal_load_dwordx4 %1, %2, %4 sc1\n\ts_waitcnt vmcnt(0)"
                     : "=&v"(a), "=&v"(b) : "v"(o_t16), "s"(src), "s"(src + 4096) : "memory");
        *(LAS u32x2*)(size_t)a_mw = (u32x2){a[0], a[1]}; *(LAS u32x2*)(size_t)(a_mw + 32) = (u32x2){a[2], a[3]};
        *(LAS u32x2*)(size_t)a_mw2 = (u32x2){b[0], b[1]}; *(LAS u32x2*)(size_t)(a_mw2 + 32) = (u32x2){b[2], b[3]};
        const unsigned mxu = max(max(max(a[0], a[1]), max(a[2], a[3])), max(max(b[0], b[1]), max(b[2], b[3])));
        if (__builtin_expect(__all(mxu != ~0u), 1)) break;
        if (spins >= cap) { cap = 0; break; }
      }
    }
    __syncthreads();
    bf16x8 mf[8], kxf[2][4], vx[2];
#pragma unroll
    for (int ks = 0; ks < 8; ++ks) mf[ks] = *(const LAS bf16x8*)(size_t)(a_mr + ks * 1024);
    auto rdk = [&](int i) {
      if constexpr (!LAST) {
        if (i < 8) kxf[i & 1][i >> 1] = *(const LAS bf16x8*)(size_t)(a_kr + ((i & 1) * 4 + (i >> 1)) * 1024);
        else vx[i - 8] = *(const bf16x8*)(lds + CL_VX + (4 * (i - 8) + w) * 1024 + lane * 16);
      }
      SB();
    };
    SB();
    qk_step<0>(aq[0], aq[1], ak[0], ak[1], mf[0], sidx);
    qk_step<1>(aq[0], aq[1], ak[0], ak[1], mf[1], sidx);
    qk_step<2>(aq[0], aq[1], ak[0], ak[1], mf[2], sidx);
    smfa<96>(aq[0], mf[3], sidx); rdk(0); smfa<104>(aq[1], mf[3], sidx); rdk(1); smfa<112>(ak[0], mf[3], sidx); rdk(2); smfa<120>(ak[1], mf[3], sidx); rdk(3);
    smfa<128>(aq[0], mf[4], sidx); rdk(4); smfa<136>(aq[1], mf[4], sidx); rdk(5); smfa<144>(ak[0], mf[4], sidx); rdk(6); smfa<152>(ak[1], mf[4], sidx); rdk(7);
    smfa<160>(aq[0], mf[5], sidx); rdk(8); smfa<168>(aq[1], mf[5], sidx); rdk(9); smfa<176>(ak[0], mf[5], sidx); smfa<184>(ak[1], mf[5], sidx);
    qk_step<6>(aq[0], aq[1], ak[0], ak[1], mf[6], sidx); qk_step<7>(aq[0], aq[1], ak[0], ak[1], mf[7], sidx);
    smf1(av[0], mf[4], wr1[0], sidx);
    {
      asm volatile("" : "+v"(aq[0]), "+v"(aq[1]));
      const unsigned q0 = pkh(aq[0]), q1 = pkh(aq[1]);
      SB(); smf1(av[1], mf[4], wr1[1], sidx);
      asm volatile("" : "+v"(ak[0]), "+v"(ak[1]));
      const f32x2 k0 = hsum(ak[0]), k1 = hsum(ak[1]);
      SB();
      *(LAS u32x2*)(size_t)a_qw = (u32x2){pk2(k0[0], k0[1]), q0}; *(LAS u32x2*)(size_t)(a_qw + 512) = (u32x2){pk2(k1[0], k1[1]), q1};
    }
    SB();
    if constexpr (!LAST) {
      asm volatile("" : "+v"(vx[0]), "+v"(vx[1]));
      SETM0(dv); smf1(av[0], mf[5], wr1[2], sidx); DMA(o_l16, vb);
      SETM0(dv + 4096); smf1(av[1], mf[5], wr1[3], sidx); DMA(o_l16, vb2);
    } else smf2(av[0], av[1], mf[5], wr1[2], wr1[3], sidx);
    smf2(av[0], av[1], mf[6], wr1[4], wr1[5], sidx);
    smf2(av[0], av[1], mf[7], wr1[6], wr1[7], sidx);
    __syncthreads();
    bf16x8 ff[4];
#pragma unroll
    for (int ks = 0; ks < 4; ++ks) {
      u32x2 a = trread_a(a_qr + ks * 1024), b = trread_a(a_qr + ks * 1024 + 128);
      ff[ks] = mk8(a[0], a[1], b[0], b[1]);
      SB();
    }
    auto publish_kv = [&]() {
      const unsigned okv = o_kv;
      if (jh == 0) {
        if (w == 2 && (c & 2) == 0)
          asm volatile("global_store_dwordx4 %0, %1, %5 sc1\n\tglobal_store_dwordx4 %0, %2, %5 offset:64 sc1\n\t"
                       "global_store_dwordx4 %0, %3, %5 offset:128 sc1\n\tglobal_store_dwordx4 %0, %4, %5 offset:192 sc1\n\ts_nop 1"
                       ::"v"(okv), "v"(ff[0]), "v"(ff[1]), "v"(ff[2]), "v"(ff[3]), "s"(ws + kmblk(s, h, pol13)) : "memory");
      } else {
        asm volatile("global_store_dword %0, %1, %3 sc1\n\tglobal_store_dword %0, %2, %3 offset:64 sc1"
                     ::"v"(okv), "v"(pkh(av[0])), "v"(pkh(av[1])), "s"(ws + kmblk(s, h, pol13)) : "memory");
      }
    };
    if constexpr (LAST) {
      smf2(av[0], av[1], mf[0], wvr[0][0], wvr[0][1], sidx);
      smf2(av[0], av[1], mf[1], wvr[1][0], wvr[1][1], sidx);
      smf2(av[0], av[1], mf[2], wvr[2][0], wvr[2][1], sidx);
      smf2(av[0], av[1], mf[3], wvr[3][0], wvr[3][1], sidx);
      asm volatile("s_nop 7" : "+v"(av[0]), "+v"(av[1]));
      publish_kv();
      asm volatile("s_waitcnt vmcnt(0)" ::: "memory");
      __syncthreads();
      if (w == 3) asm volatile("global_store_dword %0, %1, %2 sc1" ::"v"(zero_off), "v"((unsigned)(s + 1)), "s"(chflag) : "memory");
      return;
    }
    f32x4 sm[3];
    SETM0(dk); smf1(av[0], mf[0], wvr[0][0], sidx); DMA(o_l16, kb); ldw1(16384, 0, 0);
    SETM0(dk2); smf1(av[1], mf[0], wvr[0][1], sidx); DMA(o_l16, kb2); ldw1(16384, 0, 1);
    mf32z(sm[1], kxf[0][0], ff[0]); ldw1(16384, 2, 0); mf32z(sm[2], kxf[1][0], ff[0]); ldw1(16384, 2, 1); mf32z(sm[0], ff[0], ff[0]); ldw1(16384, 1, 0);
    asm volatile("" : "+v"(ff[1]), "+v"(ff[2]), "+v"(ff[3]));
    mf32(sm[1], kxf[0][1], ff[1]); ldw1(16384, 1, 1); ldw1(16384, 3, 0); mf32(sm[2], kxf[1][1], ff[1]); ldw1(16384, 3, 1); mf32(sm[0], ff[1], ff[1]); ldw1(16384, 4, 0); ldw1(16384, 4, 1);
    mf32(sm[1], kxf[0][2], ff[2]); ldw1(16384, 6, 0); mf32(sm[2], kxf[1][2], ff[2]); ldw1(16384, 6, 1); mf32(sm[0], ff[2], ff[2]); ldw1(16384, 5, 0);
    mf32(sm[1], kxf[0][3], ff[3]); ldw1(16384, 5, 1); mf32(sm[2], kxf[1][3], ff[3]); ldw1(16384, 7, 0); mf32(sm[0], ff[3], ff[3]); ldw1(16384, 7, 1);
    unsigned on[6];
    float nm, t0, t1;
    asm volatile("v_smfmac_f32_16x16x64_bf16 %3, %21, %22, %24\n\t"
                 "v_max3_f32 %0, %13, %14, %15\n\tv_mov_b32 %5, 0x3f803f80\n\tv_mov_b32 %6, 0x3f803f80\n\t"
                 "v_smfmac_f32_16x16x64_bf16 %4, %21, %23, %24\n\t"
                 "v_max3_f32 %1, %16, %17, %18\n\tv_max3_f32 %2, %19, %20, %0\n\tv_max3_f32 %1, %11, %12, %1\n\tv_max_f32 %0, %1, %2\n\t"
                 "v_mov_b32 %1, %0\n\tv_mov_b32 %7, 0x3f803f80\n\tv_mov_b32 %8, 0x3f803f80\n\tv_permlane16_swap_b32 %0, %1\n\tv_max_f32 %0, %0, %1\n\t"
                 "v_mov_b32 %1, %0\n\tv_mov_b32 %9, 0x3f803f80\n\tv_mov_b32 %10, 0\n\tv_permlane32_swap_b32 %0, %1\n\tv_max_f32 %0, %0, %1\n\t"
                 "v_mul_f32 %0, %25, %0"
                 : "=&v"(nm), "=&v"(t0), "=&v"(t1), "+v"(av[0]), "+v"(av[1]), "=&v"(on[0]), "=&v"(on[1]), "=&v"(on[2]), "=&v"(on[3]), "=&v"(on[4]), "=&v"(on[5])
                 : "v"(sm[0][0]), "v"(sm[0][1]), "v"(sm[1][0]), "v"(sm[1][1]), "v"(sm[1][2]), "v"(sm[1][3]), "v"(sm[2][0]), "v"(sm[2][1]), "v"(sm[2][2]), "v"(sm[2][3]),
                   "v"(mf[1]), "v"(wvr[1][0]), "v"(wvr[1][1]), "v"(sidx), "s"(-sc), "v"(sm[0]));
    const bf16x8 onesx = mk8(on[0], on[1], on[2], on[3]);
    const u32x2 onesm = {on[4], on[5]};
    auto fm2 = [&](float a, float b) { return __builtin_elementwise_fma((f32x2){a, b}, (f32x2){sc, sc}, (f32x2){nm, nm}); };
#define EX2(y) y[0] = __builtin_amdgcn_exp2f(y[0]); y[1] = __builtin_amdgcn_exp2f(y[1])
    SB(); smf1(av[0], mf[2], wvr[2][0], sidx); SB();
    f32x2 y0 = fm2(sm[1][0], sm[1][1]), y1 = fm2(sm[1][2], sm[1][3]), y2 = fm2(sm[2][0], sm[2][1]);
    SB(); smf1(av[1], mf[2], wvr[2][1], sidx); SB();
    EX2(y0);
    f32x2 y3 = fm2(sm[2][2], sm[2][3]);
    SB(); smf1(av[0], mf[3], wvr[3][0], sidx); SB();
    EX2(y1);
    f32x2 y4 = fm2(sm[0][0], sm[0][1]);
    SB(); smf1(av[1], mf[3], wvr[3][1], sidx); SB();
    EX2(y2);
    const unsigned px0 = pk2(y0[0], y0[1]);
    SB();
    EX2(y3);
    const unsigned px1 = pk2(y1[0], y1[1]);
    SB();
    EX2(y4);
    const bf16x8 px = mk8(px0, px1, pk2(y2[0], y2[1]), pk2(y3[0], y3[1]));
    SB();
    f32x4 d[4];
    {
      f32x4 o0, o1, rs;
      u32x2 pm;
      asm volatile("v_cvt_pk_bf16_f32 %0, %2, %3\n\tv_cvt_pk_bf16_f32 %1, %2, %3" : "=&v"(pm[0]), "=&v"(pm[1]) : "v"(y4[0]), "v"(y4[1]));
      mf32z(rs, onesx, px);
      asm volatile("" : "+v"(av[0]));
      const u32x2 vm0 = {pk2(av[0][0], av[0][1]), pk2(av[0][2], av[0][3])};
      SB(); mf32z(o0, vx[0], px);
      asm volatile("" : "+v"(av[1]));
      const u32x2 vm1 = {pk2(av[1][0], av[1][1]), pk2(av[1][2], av[1][3])};
      SB(); mf32z(o1, vx[1], px);
      asm volatile("v_mfma_f32_16x16x16_bf16 %2, %6, %5, %2\n\tv_mfma_f32_16x16x16_bf16 %0, %3, %5, %0\n\tv_mfma_f32_16x16x16_bf16 %1, %4, %5, %1"
                   : "+v"(o0), "+v"(o1), "+v"(rs) : "v"(vm0), "v"(vm1), "v"(pm), "v"(onesm));
      SB();
      d[0] = (f32x4){0, 0, 0, 0}; d[1] = d[0]; d[2] = d[0];
      asm volatile("" : "+v"(rs), "+v"(d[0]), "+v"(d[1]), "+v"(d[2]));
      float inv = __builtin_amdgcn_rcpf(rs[0]);
      d[3] = (f32x4){0, 0, 0, 0};
      asm volatile("" : "+v"(o0), "+v"(o1), "+v"(inv), "+v"(d[3]));
      *(LAS u32x2*)(size_t)a_ow = (u32x2){pk2(o0[0] * inv, o0[1] * inv), pk2(o1[0] * inv, o1[1] * inv)};
      *(LAS u32x2*)(size_t)(a_ow + 32) = (u32x2){pk2(o0[2] * inv, o0[3] * inv), pk2(o1[2] * inv, o1[3] * inv)};
    }
    __syncthreads();
    {
      const bf16x8 of0 = *(const LAS bf16x8*)(size_t)a_or, of1 = *(const LAS bf16x8*)(size_t)(a_or + 1024);
      smf2(d[0], d[1], of0, wr1[0], wr1[2], sidx); smf2(d[0], d[1], of1, wr1[1], wr1[3], sidx);
      smf2(d[2], d[3], of0, wr1[4], wr1[6], sidx);
      asm volatile("" : "+v"(d[0]), "+v"(d[1]));
      const f32x2 a0 = hsum(d[0]), b0 = hsum(d[1]);
      SB(); smf1(d[2], of1, wr1[5], sidx);
      asm volatile("global_store_dwordx4 %0, %1, %2 sc1" : : "v"(o_pd), "v"((f32x4){a0[0], a0[1], b0[0], b0[1]}), "s"(slot) : "memory");
      smf1(d[3], of1, wr1[7], sidx);
      asm volatile("s_nop 3" : "+v"(d[2]));
      const f32x2 a1 = hsum(d[2]);
      SB();
      asm volatile("s_nop 2" : "+v"(d[3]));
      const f32x2 b1 = hsum(d[3]);
      asm volatile("global_store_dwordx4 %0, %1, %2 sc1\n\ts_nop 1" : : "v"(o_pd), "v"((f32x4){a1[0], a1[1], b1[0], b1[1]}), "s"(slot2) : "memory");
    }
    publish_kv();
#pragma unroll
    for (int i = 0; i < 8; ++i) ldw2(0, i);
    {
      const char* src = slot + ((unsigned)((rq + (w & 1)) * 4096) ^ pol13);
      if (w < 2) {
#pragma unroll 1
        for (int k = 0; k < DLY1; ++k) __builtin_amdgcn_s_sleep(1);
        f32x4 r0, r1, r2, r3;
        unsigned o;
        for (unsigned spins = 0;; ++spins) {
          asm volatile("global_load_dwordx4 %0, %4, %5 sc1\n\tglobal_load_dwordx4 %1, %4, %5 offset:1024 sc1\n\t"
                       "global_load_dwordx4 %2, %4, %5 offset:2048 sc1\n\tglobal_load_dwordx4 %3, %4, %5 offset:3072 sc1\n\ts_waitcnt vmcnt(0)"
                       : "=&v"(r0), "=&v"(r1), "=&v"(r2), "=&v"(r3) : "v"(o_l16), "s"(src) : "memory");
          const f32x4 x = (r0 + r1) + (r2 + r3);
          const float y0 = comb(x[0], x[2]), y1 = comb(x[1], x[3]);
          auto rr = __builtin_amdgcn_permlane16_swap(__float_as_uint(y0), __float_as_uint(y1), false, false);
          float z = __uint_as_float(rr[0]) + __uint_as_float(rr[1]);
          z += __int_as_float(__builtin_amdgcn_update_dpp(0, __float_as_int(z), 0x128, 0xf, 0xf, true));
          const float zn = __int_as_float(__builtin_amdgcn_update_dpp(0, __float_as_int(z), 0xB1, 0xf, 0xf, true));
          o = pk2(z, zn);
          if (__builtin_expect(__all(z == z), 1)) break;
          if (o == ~0u) o = 0x7fff7fffu;
          unsigned mxu = 0;
#pragma unroll
          for (int j = 0; j < 4; ++j) mxu = max(max(mxu, max(__float_as_uint(r0[j]), __float_as_uint(r1[j]))), max(__float_as_uint(r2[j]), __float_as_uint(r3[j])));
          if (__all(mxu != ~0u)) break;
          if (spins >= cap) { cap = 0; break; }
        }
        asm volatile("s_mov_b64 exec, %3\n\tglobal_store_dword %0, %1, %2 sc1\n\ts_mov_b64 exec, -1"
                     ::"v"(o_pub), "v"(o), "s"(ws + WS_MBUF + (size_t)(s + 1) * 16384 + (rq + w) * 64), "s"(0x5555555555555555ull) : "memory");
      } else {
        if ((s == 0 || (s & 3) == 2) && s + 2 < NSEG) waitkv(ws + cnt(C_KV + ((s + 2) >> 2) * NH + h, s == 0 ? hside(h) : mnear));
        asm volatile("s_waitcnt vmcnt(0)" ::: "memory");
      }
      asm volatile("s_barrier" ::: "memory");
      if (w >= 2) {
        const u32x4 zz = {~0u, ~0u, ~0u, ~0u};
        asm volatile("global_store_dwordx4 %0, %1, %2 sc1\n\tglobal_store_dwordx4 %0, %1, %2 offset:1024 sc1\n\t"
                     "global_store_dwordx4 %0, %1, %2 offset:2048 sc1\n\tglobal_store_dwordx4 %0, %1, %2 offset:3072 sc1\n\ts_nop 1" ::"v"(o_l16), "v"(zz), "s"(src) : "memory");
        if (w == 3) asm volatile("global_store_dword %0, %1, %2 sc1" ::"v"(zero_off), "v"((unsigned)(s + 1)), "s"(chflag) : "memory");
      }
    }
#pragma unroll 1
    for (int k = 0; k < DLY2; ++k) __builtin_amdgcn_s_sleep(1);
  };
#pragma unroll 1
  for (int s = 0; s < NSEG - 1; ++s) {
    step(s, std::false_type{});
  }
  step(NSEG - 1, std::true_type{});
}

DI void dma16_sc1(const void* src, unsigned dst) {
  asm volatile("s_mov_b32 m0, %1\n\ts_nop 0\n\tglobal_load_lds_dwordx4 %0, off sc1" ::"v"(src), "s"(dst) : "memory", "m0");
}
template <int N> DI void waitv() { asm volatile("s_waitcnt vmcnt(%c0)" ::"i"(N) : "memory"); }

constexpr unsigned BSPIN = 1u << 19;
constexpr unsigned STEPGAP = 208;
constexpr int FSLEEP = 4, PSLEEP = 16, NFAST = 2;
struct Pace { unsigned long long tlast; unsigned backlog, budget; };
DI void wait_multi(const char* ws, unsigned coff, unsigned target, Pace& pc, unsigned gap, const int wv, const bool fast = false) {
  if (wv == 0) {
    const char* p = ws + coff;
    const unsigned long long nb = pc.tlast + (fast ? 0u : gap);
    unsigned long long now = __builtin_amdgcn_s_memrealtime();
    bool ok = false;
    if (pc.backlog) ok = __all(ld4_sc1(p) >= target);
    if (!ok) {
      pc.backlog = 0;
      for (unsigned spins = 0; now < nb && spins < 8192u; ++spins) { __builtin_amdgcn_s_sleep(4); now = __builtin_amdgcn_s_memrealtime(); }
      for (; !__all(ld4_sc1(p) >= target) && pc.budget != 0u; --pc.budget) {
        if (fast) __builtin_amdgcn_s_sleep(FSLEEP); else __builtin_amdgcn_s_sleep(PSLEEP);
        now = __builtin_amdgcn_s_memrealtime();
      }
    }
    pc.tlast = now;
  }
  __syncthreads();
}
DI void publish_cnt(char* ws, unsigned coff, const int wv, unsigned val = 1u) {
  asm volatile("s_waitcnt vmcnt(0)" ::: "memory");
  __syncthreads();
  if (wv == 0 && lane_id() == 0) __hip_atomic_fetch_add((unsigned*)(ws + coff), val, __ATOMIC_RELAXED, __HIP_MEMORY_SCOPE_AGENT);
}
DI void publish_flag(char* ws, unsigned coff, unsigned val, const int wv, bool both = false) {
  asm volatile("s_waitcnt vmcnt(0)" ::: "memory");
  __syncthreads();
  if (wv == 0) {
    const unsigned o = coff ^ (both ? (unsigned)(lane_id() & 1) << 13 : 0u);
    asm volatile("global_store_dword %0, %1, %2 sc1" ::"v"(o), "v"(val), "s"(ws) : "memory");
  }
}
DI u32x4 pk8(f32x4 a, f32x4 b) { return (u32x4){pk2(a[0], a[1]), pk2(a[2], a[3]), pk2(b[0], b[1]), pk2(b[2], b[3])}; }
DI u32x4 bld16(__amdgpu_buffer_rsrc_t r, size_t off) { return __builtin_bit_cast(u32x4, __builtin_amdgcn_raw_buffer_load_b128(r, (unsigned)off, 0, 16)); }
DI u32x2 bld8(__amdgpu_buffer_rsrc_t r, size_t off) { return __builtin_bit_cast(u32x2, __builtin_amdgcn_raw_buffer_load_b64(r, (unsigned)off, 0, 16)); }
DI void bst16(__amdgpu_buffer_rsrc_t r, size_t off, u32x4 v) { __builtin_amdgcn_raw_buffer_store_b128(v, r, (unsigned)off, 0, 16); }
DI bf16x8 asb(u32x4 v) { return __builtin_bit_cast(bf16x8, v); }
DI float gelu_t(float x) {
  const float y = 0.7978845608028654f * (x + 0.044715f * x * x * x);
  return x * __builtin_amdgcn_rcpf(1.f + __builtin_amdgcn_exp2f(-2.f * 1.4426950408889634f * y));
}
template <int NT, int KS, class RowPtr>
DI void load_frags(bf16x8 (&wf)[NT][KS], RowPtr rowptr, char* stgp, const int colbyte[NT]) {
  const int lane = lane_id(), c = lane & 15, g = lane >> 4;
  constexpr int RB_ = NT * 32, RPP = 1024 / RB_, NP = KS * 32 / RPP;
  const unsigned stg = (unsigned)(size_t)(LAS char*)stgp;
#pragma unroll
  for (int i = 0; i < NP; ++i) dma16(rowptr(i * RPP + lane / (2 * NT), lane % (2 * NT)), stg + i * 1024);
  waitv<0>();
  const char* ta = stgp + (8 * g + (c >> 2)) * RB_;
#pragma unroll
  for (int n = 0; n < NT; ++n)
#pragma unroll
    for (int ks = 0; ks < KS; ++ks) {
      u32x2 lo = trread(ta + ks * 32 * RB_ + colbyte[n]), hi = trread(ta + (ks * 32 + 4) * RB_ + colbyte[n]);
      wf[n][ks] = mk8(lo[0], lo[1], hi[0], hi[1]);
    }
  asm volatile("s_waitcnt lgkmcnt(0)" ::: "memory");
}

template <int NT, int KS, int RS, int TMASK = 0, int MI = 2>
DI void seg_gemm(const bf16x8 (&wf)[NT][KS], const char* img, f32x4 (&acc)[MI][NT]) {
  const int lane = lane_id(), c = lane & 15, g = lane >> 4;
  constexpr int BT = 4, NB = KS / BT;
  static_assert(KS % BT == 0, "k-steps");
  const char* base = img + c * RS;
  bf16x8 af[2][BT][MI];
  auto rd = [&](int b, bf16x8 (&d)[BT][MI]) {
#pragma unroll
    for (int k = 0; k < BT; ++k)
#pragma unroll
      for (int mi = 0; mi < MI; ++mi) d[k][mi] = *(const bf16x8*)(base + mi * 16 * RS + b * 256 + (((k * 4 + g) ^ c) << 4));
  };
#pragma unroll
  for (int i = 0; i < MI; ++i)
#pragma unroll
    for (int n = 0; n < NT; ++n) acc[i][n] = (f32x4){0, 0, 0, 0};
  rd(0, af[0]);
#pragma unroll
  for (int b = 0; b < NB; ++b) {
    if (b + 1 < NB) rd(b + 1, af[(b + 1) & 1]);
    __builtin_amdgcn_sched_barrier(0);
#pragma unroll
    for (int k = 0; k < BT; ++k)
#pragma unroll
      for (int mi = 0; mi < MI; ++mi)
#pragma unroll
        for (int n = 0; n < NT; ++n)
          acc[mi][n] = ((TMASK >> n) & 1) ? mfma16(af[b & 1][k][mi], wf[n][b * BT + k], acc[mi][n]) : mfma16(wf[n][b * BT + k], af[b & 1][k][mi], acc[mi][n]);
    __builtin_amdgcn_sched_barrier(0);
  }
}

constexpr int NCHAIN = 64, NBULK = 192;
constexpr int PJ0 = 144;

DI void stage_xn(const char* ws, int sg, unsigned dst, int w, int lane) {
#pragma unroll
  for (int i = 0; i < 8; ++i) {
    const int r = w * 8 + i;
    dma16(ws + WS_XN + ((size_t)(sg * 32 + r) * DM) * 2 + ((lane ^ (r & 15)) << 4), dst + r * 1024);
  }
}

DI void early_kv(int e, const bf16* __restrict__ Wq, const bf16* __restrict__ Wk, const bf16* __restrict__ Wv, const bf16* __restrict__ Wo, char* ws, char* lds, __amdgpu_buffer_rsrc_t R, const int wv) {
  const int lane = lane_id(), w = wv, c = lane & 15, g = lane >> 4;
  const int h = (e & 7) + 8 * ((e >> 3) & 3), isv = (e >> 5) & 1, half = e >> 6, d0 = half * 64 + w * 16;
  const unsigned ldsbase = (unsigned)(size_t)(LAS char*)lds;
  {
    const int u = (e >> 3) * 256 + w * 64 + lane;
    const size_t o = (size_t)(u >> 3) * HD + ((e & 7) + 8 * ((u >> 1) & 3)) * 128 + (u & 1) * 64, o2 = ((size_t)((e & 7) + 8 * (u >> 10)) * 128 * DM) + (size_t)(u & 1023) * 64;
    const bf16* pp[4] = {Wk + o, Wv + o, Wq + o, Wo + o2};
#pragma unroll
    for (int i = 0; i < 4; ++i) asm volatile("s_mov_b32 m0, %1\n\ts_nop 0\n\tglobal_load_lds_dword %0, off" ::"v"(pp[i]), "s"(ldsbase + 131072 + w * 256) : "memory", "m0");
  }
  bf16x8 wf[1][16];
  {
    const bf16* base = (isv ? Wv : Wk) + h * 128 + half * 64;
#pragma unroll
    for (int i = 0; i < 16; ++i) {
      const int r = w * 128 + i * 8 + (lane >> 3);
      dma16(base + (size_t)r * HD + (((lane & 7) ^ (((r >> 1) & 3) << 1)) << 3), ldsbase + (w * 16 + i) * 1024);
    }
    stage_xn(ws, 0, ldsbase + 65536, w, lane); stage_xn(ws, 1, ldsbase + 98304, w, lane);
    waitv<0>();
    __syncthreads();
#pragma unroll
    for (int ks = 0; ks < 16; ++ks) {
      const int r0 = ks * 32 + 8 * g + (c >> 2), r1 = r0 + 4;
      u32x2 lo = trread(lds + r0 * 128 + ((w ^ ((r0 >> 1) & 3)) << 5) + (c & 3) * 8), hi = trread(lds + r1 * 128 + ((w ^ ((r1 >> 1) & 3)) << 5) + (c & 3) * 8);
      wf[0][ks] = mk8(lo[0], lo[1], hi[0], hi[1]);
    }
  }
#pragma unroll 1
  for (int sg = 0; sg < 4; ++sg) {
    f32x4 acc[2][1];
    const char* img = lds + ((sg + 2) & 3) * 32768;
    if (isv) {
      seg_gemm<1, 16, 1024, 1>(wf, img, acc);
      bst16(R, blk(WS_VT, sg, h, 0u) + ((2 * (d0 >> 5) + ((c >> 2) & 1)) * 64 + g * 16 + (2 * (w & 1) + (c >> 3)) * 4 + (c & 3)) * 16, pk8(acc[0][0], acc[1][0]));
    } else {
      seg_gemm<1, 16, 1024, 0>(wf, img, acc);
#pragma unroll
      for (int mi = 0; mi < 2; ++mi)
        __builtin_amdgcn_raw_buffer_store_b64((u32x2){pk2(acc[mi][0][0], acc[mi][0][1]), pk2(acc[mi][0][2], acc[mi][0][3])}, R,
                                              blk(WS_K, sg, h, 0u) + ((mi * 4 + (d0 >> 5)) * 64 + (2 * (w & 1) + (g >> 1)) * 16 + c) * 16 + (g & 1) * 8, 0, 16);
    }
    if (sg == 0) { __syncthreads(); stage_xn(ws, 2, ldsbase, w, lane); stage_xn(ws, 3, ldsbase + 32768, w, lane); }
    if (sg == 1) publish_cnt(ws, cnt(C_E0 + h, hside(h)), wv);
  }
  publish_cnt(ws, cnt(C_KV + h, hside(h)), wv);
}

template <bool KV>
DI void pj_role(int id, const bf16* __restrict__ W0, const bf16* __restrict__ W1, char* ws, char* lds, __amdgpu_buffer_rsrc_t R, const int wv) {
  const int lane = lane_id(), w = wv, c = lane & 15, g = lane >> 4;
  const int h0 = KV ? id : (id & 7) + 16 * (id >> 3), h1 = KV ? id : h0 + 8;
  const unsigned ldsbase = (unsigned)(size_t)(LAS char*)lds;
  bf16x8 wf[4][16];
  {
    bf16x8 (&lo)[2][16] = *reinterpret_cast<bf16x8 (*)[2][16]>(&wf[0][0]);
    bf16x8 (&hi)[2][16] = *reinterpret_cast<bf16x8 (*)[2][16]>(&wf[2][0]);
    const int cp[2] = {(c & 3) * 16, (c & 3) * 16 + 8};
    const int cn[2] = {(c & 3) * 8, 32 + (c & 3) * 8};
    const bf16* b0 = W0 + h0 * 128 + w * 32; const bf16* b1 = W1 + h1 * 128 + w * 32;
    load_frags<2, 16>(lo, [&](int r, int pc) { return b0 + (size_t)r * HD + pc * 8; }, lds + w * 32768, cp);
    load_frags<2, 16>(hi, [&](int r, int pc) { return b1 + (size_t)r * HD + pc * 8; }, lds + w * 32768, KV ? cn : cp);
  }
  unsigned cap = BSPIN;
  const unsigned pol13 = get_pol(ws, cap);
  __syncthreads();
  constexpr int S0 = KV ? 4 : 0;
  auto pub = [&](int rb) {
    if (w == 3 && lane == 0)
      __hip_atomic_fetch_add((unsigned*)(ws + cnt(KV ? C_KV + rb * NH + h0 : C_Q + rb * 8 + (h0 & 7), hside(h0) ^ pol13)), KV ? 4u : 2u, __ATOMIC_RELAXED, __HIP_MEMORY_SCOPE_AGENT);
  };
  Pace pc = {0, 1, BSPIN};
  stage_xn(ws, S0, ldsbase, w, lane); stage_xn(ws, S0 + 1, ldsbase + 32768, w, lane);
#pragma unroll 1
  for (int sg = S0; sg < NSEG; ++sg) {
    const bool pb = (sg & 3) == 0 && sg > S0;
    if (sg + 1 < NSEG && !pb) { if (sg > S0) waitv<12>(); else waitv<8>(); } else waitv<0>();
    __syncthreads();
    if (pb) pub((sg >> 2) - 1);
    if ((sg & 3) == 0 && sg >= QRING) wait_multi(ws, cnt(C_WO + lane, hside(h0) ^ pol13), (unsigned)(sg - QRING + (lane >> 4) + 1), pc, 4 * STEPGAP, wv);
    if (sg + 2 < NSEG) stage_xn(ws, sg + 2, ldsbase + ((sg + 2 - S0) % 3) * 32768, w, lane);
    f32x4 acc[2][4];
    seg_gemm<4, 16, 1024, KV ? 12 : 0>(wf, lds + ((sg - S0) % 3) * 32768, acc);
#pragma unroll
    for (int mi = 0; mi < 2; ++mi)
      bst16(R, blk(KV ? WS_K : WS_Q, sg, h0, pol13) + ((mi * 4 + w) * 64 + lane) * 16, pk8(acc[mi][0], acc[mi][1]));
    if (KV) {
#pragma unroll
      for (int n = 0; n < 2; ++n)
        bst16(R, blk(WS_VT, sg, h1, pol13) + ((2 * w + ((c >> 2) & 1)) * 64 + g * 16 + (2 * n + (c >> 3)) * 4 + (c & 3)) * 16, pk8(acc[0][2 + n], acc[1][2 + n]));
    } else {
#pragma unroll
      for (int mi = 0; mi < 2; ++mi)
        bst16(R, blk(WS_Q, sg, h1, pol13) + ((mi * 4 + w) * 64 + lane) * 16, pk8(acc[mi][2], acc[mi][3]));
    }
  }
  asm volatile("s_waitcnt vmcnt(0)" ::: "memory");
  __syncthreads();
  pub(NRB - 1);
}

DI void aw_role(int id, const bf16* __restrict__ Wo, char* ws, char* lds, __amdgpu_buffer_rsrc_t R, const int wv) {
  const int lane = lane_id(), w = wv, t = w * 64 + lane, c = lane & 15, g = lane >> 4;
  const int par = id >> 4, hg = id & 7, nt = (id >> 3) & 1, h = hg + 8 * w;
  bf16x8 wf[4][16];
  {
    bf16x8 (&lo)[2][16] = *reinterpret_cast<bf16x8 (*)[2][16]>(&wf[0][0]);
    bf16x8 (&hi)[2][16] = *reinterpret_cast<bf16x8 (*)[2][16]>(&wf[2][0]);
    const int cb[2] = {(c & 3) * 8, 32 + (c & 3) * 8};
    const bf16* base = Wo + (size_t)hg * 128 * DM + nt * 256 + w * 64;
    load_frags<2, 16>(lo, [&](int r, int pc) { return base + (size_t)((r >> 7) * 1024 + (r & 127)) * DM + pc * 8; }, lds + w * 32768, cb);
    load_frags<2, 16>(hi, [&](int r, int pc) { return base + (size_t)((r >> 7) * 1024 + (r & 127)) * DM + 32 + pc * 8; }, lds + w * 32768, cb);
  }
  unsigned cap = BSPIN;
  const unsigned pol13 = get_pol(ws, cap), sp = hside(hg) ^ pol13;
  __syncthreads();
  const float sc = 0.08838834764831845f * 1.4426950408889634f;
  Pace tl0 = {0, 1, BSPIN}, tl1 = {0, 1, BSPIN};
#pragma unroll 1
  for (int s = par; s < NSEG; s += 4) {
    const int rb = s >> 2;
    wait_multi(ws, lane < 4 ? cnt(C_KV + rb * NH + hg + 8 * lane, rb ? sp : hside(hg)) : lane >= 8 && lane < 16 ? cnt(C_LN + (s & 1) * 8 + (lane & 7), hside(lane) ^ pol13) : cnt(C_Q + rb * 8 + hg, sp),
               lane >= 8 && lane < 16 ? (unsigned)max(s - PRING + 1, 0) : 4u, tl0, 0u, wv);
    f32x4 st[3][2];
#pragma unroll
    for (int j = 0; j < 3; ++j) { st[j][0] = (f32x4){0, 0, 0, 0}; st[j][1] = st[j][0]; }
    bf16x8 qf[2][4]; u32x4 vxr[8];
    {
      bf16x8 kf[2][4];
#pragma unroll
      for (int ks = 0; ks < 4; ++ks) {
#pragma unroll
        for (int x = 0; x < 2; ++x) {
          qf[x][ks] = asb(bld16(R, blk(WS_Q, s, h, pol13) + ((x * 4 + ks) * 64 + lane) * 16));
          kf[x][ks] = asb(bld16(R, blk(WS_K, s, h, pol13) + ((x * 4 + ks) * 64 + lane) * 16));
        }
      }
#pragma unroll
      for (int dt = 0; dt < 8; ++dt) vxr[dt] = bld16(R, blk(WS_VT, s, h, pol13) + (dt * 64 + lane) * 16);
#pragma unroll
      for (int ks = 0; ks < 4; ++ks) {
        st[1][0] = mfma16(kf[0][ks], qf[0][ks], st[1][0]); st[1][1] = mfma16(kf[0][ks], qf[1][ks], st[1][1]);
        st[2][1] = mfma16(kf[1][ks], qf[1][ks], st[2][1]);
      }
    }
    wait_multi(ws, cnt(C_CH + 2 * (hg + 8 * ((lane >> 1) & 3)) + (lane & 1), sp), (unsigned)(s + 1), tl1, 4 * STEPGAP, wv, s >= NSEG - NFAST);
    u32x2 vmr[8];
    {
      bf16x8 kmf[4];
#pragma unroll
      for (int ks = 0; ks < 4; ++ks) kmf[ks] = asb(bld16(R, kmblk(s, h, pol13) + (c & 7) * 256 + ks * 64 + 16 * g));
#pragma unroll
      for (int dt = 0; dt < 8; ++dt) vmr[dt] = bld8(R, kmblk(s, h, pol13) + 2048 + ((dt >> 1) * 32 + 8 * (c >> 2) + 4 * (dt & 1) + (c & 3)) * 16 + (g & 1) * 8);
#pragma unroll
      for (int ks = 0; ks < 4; ++ks) { st[0][0] = mfma16(kmf[ks], qf[0][ks], st[0][0]); st[0][1] = mfma16(kmf[ks], qf[1][ks], st[0][1]); }
    }
    bf16x8 px[2], pm[2];
#pragma unroll
    for (int qt = 0; qt < 2; ++qt) {
      float v[12];
#pragma unroll
      for (int r = 0; r < 4; ++r) {
        v[r] = (g < 2) ? st[0][qt][r] : -INFINITY;
        const int kj = 4 * g + r;
        v[4 + r] = (qt == 1 || kj <= c) ? st[1][qt][r] : -INFINITY;
        v[8 + r] = (qt == 1 && kj <= c) ? st[2][qt][r] : -INFINITY;
      }
      float mx = v[0];
#pragma unroll
      for (int i = 1; i < 12; ++i) mx = fmaxf(mx, v[i]);
      mx = xmax16(mx); mx = xmax32(mx);
      float sum = 0;
#pragma unroll
      for (int i = 0; i < 12; ++i) { v[i] = __builtin_amdgcn_exp2f((v[i] - mx) * sc); sum += v[i]; }
      sum = xsum16(sum); sum = xsum32(sum);
      const float inv = __builtin_amdgcn_rcpf(sum);
      px[qt] = mk8(pk2(v[4] * inv, v[5] * inv), pk2(v[6] * inv, v[7] * inv), pk2(v[8] * inv, v[9] * inv), pk2(v[10] * inv, v[11] * inv));
      pm[qt] = mk8(pk2(v[0] * inv, v[1] * inv), pk2(v[2] * inv, v[3] * inv), 0u, 0u);
    }
#pragma unroll
    for (int dj = 0; dj < 4; ++dj)
#pragma unroll
      for (int qt = 0; qt < 2; ++qt) {
        f32x4 o[2];
#pragma unroll
        for (int e = 0; e < 2; ++e) {
          o[e] = mfma16(asb(vxr[2 * dj + e]), px[qt], (f32x4){0, 0, 0, 0});
          o[e] = mfma16(mk8(vmr[2 * dj + e][0], vmr[2 * dj + e][1], 0u, 0u), pm[qt], o[e]);
        }
        *(u32x4*)(lds + (qt * 16 + c) * 1024 + (((w * 16 + dj * 4 + g) ^ c) << 4)) = pk8(o[0], o[1]);
      }
    __syncthreads();
    f32x4 acc[2][4];
    seg_gemm<4, 16, 1024>(wf, lds, acc);
#pragma unroll
    for (int mi = 0; mi < 2; ++mi)
#pragma unroll
      for (int n = 0; n < 4; ++n)
        bst16(R, nz((unsigned)WS_P + (s & (PRING - 1)) * 524288 + (c >> 2) * 131072 + hg * 16384, (unsigned)(mi << 13) ^ pol13) + (nt * 16 + w * 4 + n) * 256 + (c & 3) * 64 + g * 16,
              __builtin_bit_cast(u32x4, acc[mi][n]));
    publish_flag(ws, cnt(C_WO + id, 0u), (unsigned)(s + 1), wv, true);
  }
}

DI void ln_role(int id, int par, const bf16* __restrict__ x, char* ws, char* lds, __amdgpu_buffer_rsrc_t R, const int wv) {
  const int lane = lane_id(), w = wv;
  {
    const int v = (par * 8 + id) * 4 + w;
    const char* p = ws + WS_PART + (size_t)v * 16384 + (v & 3) * 2048;
    unsigned best[2] = {~0u, ~0u};
    const unsigned z = ~0u, zo = 0u;
#pragma unroll 1
    for (int i = 0; i < NVIT; ++i) {
      const int b = (i ^ v) & 1;
      unsigned long long t0, t1;
      asm volatile("s_memtime %0\n\ts_waitcnt lgkmcnt(0)\n\tglobal_store_dword %2, %3, %4 sc1\n\ts_waitcnt vmcnt(0)\n\ts_memtime %1\n\ts_waitcnt lgkmcnt(0)"
                   : "=&s"(t0), "=&s"(t1) : "v"(zo), "v"(z), "s"(p + b * 8192) : "memory");
      const unsigned d = (unsigned)(t1 - t0);
      if (b) best[1] = min(best[1], d); else best[0] = min(best[0], d);
    }
    int df = (int)(best[0] - best[1]);
    df = min(max(df, -256), 256);
    const float vote = ((id >> 2) & 1 ? -(float)df : (float)df) + 0.25f;
    asm volatile("global_store_dword %0, %1, %2 sc1" ::"v"(zo), "v"(vote), "s"(ws + WS_VOTE + v * 64) : "memory");
  }
  unsigned cap = BSPIN;
  const unsigned pol13 = get_pol(ws, cap), sp = hside(id) ^ pol13;
  Pace tl = {0, 1, BSPIN};
#pragma unroll 1
  for (int s = par; s < NSEG; s += 2) {
    const int r32 = id * 4 + w, rr = (lane >> 2) & 3, col = (8 * w + (lane >> 4)) * 16 + (lane & 3) * 4;
    const bf16* xp = x + (size_t)(s * 32 + id * 4 + rr) * DM + col;
    const u32x2 v0 = *(const u32x2*)xp, v1 = *(const u32x2*)(xp + 64);
    wait_multi(ws, cnt(C_WO + (s & 3) * 16 + (lane & 15), sp), (unsigned)(s + 1), tl, 2 * STEPGAP, wv, s >= NSEG - NFAST);
    f32x4 p[SKW][2];
#pragma unroll
    for (int sk = 0; sk < SKW; ++sk) {
      const unsigned o = nz((unsigned)WS_P + (s & (PRING - 1)) * 524288 + (id & 3) * 131072 + sk * 16384, sp) + w * 2048 + lane * 16;
      p[sk][0] = __builtin_bit_cast(f32x4, bld16(R, o)); p[sk][1] = __builtin_bit_cast(f32x4, bld16(R, o + 1024));
    }
    {
      const f32x4 a0 = ((p[0][0] + p[1][0]) + (p[2][0] + p[3][0])) + ((p[4][0] + p[5][0]) + (p[6][0] + p[7][0]));
      const f32x4 a1 = ((p[0][1] + p[1][1]) + (p[2][1] + p[3][1])) + ((p[4][1] + p[5][1]) + (p[6][1] + p[7][1]));
      const u32x4 ar = pk8(a0, a1);
      char* d = lds + rr * 1040 + col * 2;
      *(u32x2*)d = (u32x2){pk2(bflo(v0[0]) + bflo(ar[0]), bfhi(v0[0]) + bfhi(ar[0])), pk2(bflo(v0[1]) + bflo(ar[1]), bfhi(v0[1]) + bfhi(ar[1]))};
      *(u32x2*)(d + 128) = (u32x2){pk2(bflo(v1[0]) + bflo(ar[2]), bfhi(v1[0]) + bfhi(ar[2])), pk2(bflo(v1[1]) + bflo(ar[3]), bfhi(v1[1]) + bfhi(ar[3]))};
    }
    __syncthreads();
    const u32x4 hb = *(const u32x4*)(lds + w * 1040 + lane * 16);
    float f[8] = {bflo(hb[0]), bfhi(hb[0]), bflo(hb[1]), bfhi(hb[1]), bflo(hb[2]), bfhi(hb[2]), bflo(hb[3]), bfhi(hb[3])};
    const float mean = wave_sum(((f[0] + f[1]) + (f[2] + f[3])) + ((f[4] + f[5]) + (f[6] + f[7]))) * (1.f / DM);
    float qq = 0;
#pragma unroll
    for (int i = 0; i < 8; ++i) { f[i] -= mean; qq += f[i] * f[i]; }
    const float rs = 1.f / sqrtf(wave_sum(qq) * (1.f / DM) + 1e-5f);
    u32x4 o = {pk2(f[0] * rs, f[1] * rs), pk2(f[2] * rs, f[3] * rs), pk2(f[4] * rs, f[5] * rs), pk2(f[6] * rs, f[7] * rs)};
    bst16(R, rowoff(WS_HN, s, r32, pol13) + lane * 16, o);
    bst16(R, rowoff(WS_H, s, r32, pol13) + lane * 16, hb);
    publish_flag(ws, cnt(C_LN + par * 8 + id, sp), (unsigned)(s + 1), wv);
  }
}

DI void f1_role(int id, const bf16* __restrict__ Wg, const bf16* __restrict__ Wu, char* ws, char* lds, __amdgpu_buffer_rsrc_t R, const int wv) {
  const int lane = lane_id(), w = wv, t = w * 64 + lane, c = lane & 15, g = lane >> 4;
  const int par = id / NFS, fs = id % NFS;
  const int f0 = fs * 64 + w * 16;
  const bool valid = f0 < DF;
  bf16x8 wgu[2][16];
  {
    const int cb[2] = {(c & 3) * 8, 32 + (c & 3) * 8};
    const int fc = valid ? f0 : 0;
    load_frags<2, 16>(wgu, [&](int r, int pc) { return (pc < 2 ? Wg : Wu) + (size_t)r * DF + fc + (pc & 1) * 8; }, lds + w * 32768, cb);
  }
  unsigned cap = BSPIN;
  const unsigned pol13 = get_pol(ws, cap), sp = (unsigned)(par << 13) ^ pol13;
  __syncthreads();
  const unsigned ldsbase = (unsigned)(size_t)(LAS char*)lds;
  Pace tl = {0, 1, BSPIN};
#pragma unroll 1
  for (int s = 0; s < NSEG; ++s) {
    wait_multi(ws, cnt(C_LN + (s & 1) * 8 + par * 4 + (lane & 3), sp), (unsigned)(s + 1), tl, STEPGAP, wv, s >= NSEG - NFAST - 1);
#pragma unroll
    for (int i = 0; i < 4; ++i) {
      const int r = w * 4 + i;
      dma16_sc1(ws + rowoff(WS_HN, s, par * 16 + r, pol13) + ((lane ^ r) << 4), ldsbase + r * 1024);
    }
    waitv<0>();
    __syncthreads();
    f32x4 acc[1][2];
    seg_gemm<2, 16, 1024, 0, 1>(wgu, lds, acc);
    float r[4];
#pragma unroll
    for (int j = 0; j < 4; ++j) r[j] = valid ? gelu_t(acc[0][0][j]) * acc[0][1][j] : 0.f;
    __builtin_amdgcn_raw_buffer_store_b64((u32x2){pk2(r[0], r[1]), pk2(r[2], r[3])}, R, actoff(s, par, (unsigned)(c * DFP + f0 + 4 * g) * 2u, pol13), 0, 16);
    publish_flag(ws, cnt(C_FF + fs, sp), (unsigned)(s + 1), wv);
  }
}

DI void f2_role(int id, const bf16* __restrict__ Wd, char* ws, bf16* __restrict__ out, char* lds, __amdgpu_buffer_rsrc_t R, const int wv) {
  const int lane = lane_id(), w = wv, t = w * 64 + lane, c = lane & 15, g = lane >> 4;
  const int par = id >> 3, nb = id & 7, n0 = nb * 64 + w * 16;
  bf16x8 wd[1][44];
  {
    const int cb[1] = {(c & 3) * 8};
    bf16x8 (&lo)[1][22] = *reinterpret_cast<bf16x8 (*)[1][22]>(&wd[0][0]);
    bf16x8 (&hi)[1][22] = *reinterpret_cast<bf16x8 (*)[1][22]>(&wd[0][22]);
    load_frags<1, 22>(lo, [&](int r, int pc) { return Wd + (size_t)r * DM + n0 + pc * 8; }, lds + w * 32768, cb);
    load_frags<1, 22>(hi, [&](int r, int pc) { int fr = 704 + r; fr = fr < DF ? fr : DF - 1; return Wd + (size_t)fr * DM + n0 + pc * 8; }, lds + w * 32768, cb);
  }
  unsigned cap = BSPIN;
  const unsigned pol13 = get_pol(ws, cap), sp = (unsigned)(par << 13) ^ pol13;
  __syncthreads();
  const unsigned ldsbase = (unsigned)(size_t)(LAS char*)lds;
  unsigned soff[11];
#pragma unroll
  for (int i = 0; i < 11; ++i) {
    const int u = (w * 11 + i) * 64 + lane, r = u / 176, sl = u % 176;
    soff[i] = r * (DFP * 2) + (((sl & ~15) | ((sl ^ r) & 15)) << 4);
  }
  Pace tl = {0, 1, BSPIN};
#pragma unroll 1
  for (int s = 0; s < NSEG; ++s) {
    wait_multi(ws, cnt(C_FF + min(lane, NFS - 1), sp), (unsigned)(s + 1), tl, STEPGAP, wv, s >= NSEG - NFAST - 2);
    const int row0 = s * 32 + par * 16;
#pragma unroll
    for (int i = 0; i < 11; ++i) dma16_sc1(ws + actoff(s, par, soff[i], pol13), ldsbase + (w * 11 + i) * 1024);
    const u32x2 hv = bld8(R, rowoff(WS_H, s, par * 16 + c, pol13) + (n0 + 4 * g) * 2);
    waitv<0>();
    __syncthreads();
    f32x4 acc[1][1];
    seg_gemm<1, 44, DFP * 2, 0, 1>(wd, lds, acc);
    const unsigned d0 = pk2(acc[0][0][0], acc[0][0][1]), d1 = pk2(acc[0][0][2], acc[0][0][3]);
    u32x2 o = {pk2(bflo(hv[0]) + bflo(d0), bfhi(hv[0]) + bfhi(d0)), pk2(bflo(hv[1]) + bflo(d1), bfhi(hv[1]) + bfhi(d1))};
    *(u32x2*)(out + (size_t)(row0 + c) * DM + n0 + 4 * g) = o;
  }
}

__global__ void __launch_bounds__(256, 1) mega_kernel(const bf16* __restrict__ x, const bf16* __restrict__ Wq, const bf16* __restrict__ Wk, const bf16* __restrict__ Wv,
                                                      const bf16* __restrict__ Wo, const bf16* __restrict__ Wg, const bf16* __restrict__ Wu, const bf16* __restrict__ Wd,
                                                      char* ws, bf16* __restrict__ out) {
  extern __shared__ __attribute__((aligned(16))) char lds[];
  const int wv = __builtin_amdgcn_readfirstlane(threadIdx.x >> 6);
  ws = al2m(ws);
  if (blockIdx.x < NCHAIN) { chain_role(Wq, Wk, Wv, Wo, ws, lds, wv); return; }
  const int b = blockIdx.x - NCHAIN;
  const __amdgpu_buffer_rsrc_t R = __builtin_amdgcn_make_buffer_rsrc(ws, 0, (int)WS_END, 0x00020000);
  if (b >= PJ0) {
    if (b < PJ0 + 32) pj_role<true>(b - PJ0, Wk, Wv, ws, lds, R, wv);
    else pj_role<false>(b - PJ0 - 32, Wq, Wq, ws, lds, R, wv);
    return;
  }
  if (b < 128) early_kv(b, Wq, Wk, Wv, Wo, ws, lds, R, wv);
  const int xg = b & 7, slot = b >> 3;
  if (slot < 8) aw_role(b, Wo, ws, lds, R, wv);
  else if (slot < 16) {
    if ((xg & 3) == 0) f2_role((xg >> 2) * 8 + (slot - 8), Wd, ws, out, lds, R, wv);
    else {
      const int fs = ((xg & 3) - 1) * 8 + (slot - 8);
      if (fs < NFS) f1_role((xg >> 2) * NFS + fs, Wg, Wu, ws, lds, R, wv);
    }
  } else ln_role(xg, slot - 16, x, ws, lds, R, wv);
}

extern "C" void kernel_launch(void* const* d_in, const int* in_sizes, int n_in, void* d_out, int out_size, void* d_ws, size_t ws_size,
                              hipStream_t stream) {
  const bf16* x = (const bf16*)d_in[0];
  const bf16* mem0 = (const bf16*)d_in[1];
  const bf16* Wq = (const bf16*)d_in[2];
  const bf16* Wk = (const bf16*)d_in[3];
  const bf16* Wv = (const bf16*)d_in[4];
  const bf16* Wo = (const bf16*)d_in[5];
  const bf16* Wg = (const bf16*)d_in[8];
  const bf16* Wu = (const bf16*)d_in[9];
  const bf16* Wd = (const bf16*)d_in[10];
  char* ws = (char*)d_ws;
  bf16* out = (bf16*)d_out;
  (void)hipFuncSetAttribute((const void*)mega_kernel, hipFuncAttributeMaxDynamicSharedMemorySize, CL_END);
  prep_kernel<<<512, 256, 0, stream>>>(x, mem0, ws);
  mega_kernel<<<NCHAIN + NBULK, 256, CL_END, stream>>>(x, Wq, Wk, Wv, Wo, Wg, Wu, Wd, ws, out);
}
```
